```python
import torch

def _softmax(x, axis=-1):
    return torch.softmax(x, dim=axis)
def _gelu(x):
    return torch.nn.functional.gelu(x, approximate="tanh")
def _silu(x):
    return torch.nn.functional.silu(x)
def _sigmoid(x):
    return torch.sigmoid(x)
def _layernorm(x, axis=-1, eps=1e-5):
    m = x.mean(dim=axis, keepdim=True)
    v = x.var(dim=axis, keepdim=True, unbiased=False)
    return (x - m) / torch.sqrt(v + eps)
def _scatter_add(tgt, idx, src, axis=0):
    return tgt.clone().index_add_(axis, idx, src)
def _segment_sum(src, seg, n):
    out = torch.zeros((n,) + src.shape[1:], dtype=src.dtype)
    return out.index_add_(0, seg, src)
def _pad(x, pw):
    flat = tuple(v for pair in reversed(pw) for v in pair)
    return torch.nn.functional.pad(x, flat)

kernel_name = 'sequential_preLN__sliding_block_cached__dense_activation_gelu__decode'

def setup_inputs():

    _w = (lambda a: (lambda t: t.to(torch.bfloat16) if t.is_floating_point() else t)(torch.as_tensor(a)))
    def _A_setup():
        B, Sq, D = 2, 1, 512
        Hq, Hkv, Sk, Dh = 32, 8, 8192, 128
        assert Hq % Hkv == 0, (Hq, Hkv)
        return {
            "x": _w((lambda *s: torch.randn(*s, dtype=torch.bfloat16) * 0.05)(B, Sq, D)),
            "k_cache": _w((lambda *s: torch.randn(*s, dtype=torch.bfloat16) * 3.2)(B, Hkv, Sk, Dh)),
            "v_cache": _w((lambda *s: torch.randn(*s, dtype=torch.bfloat16) * 0.8)(B, Hkv, Sk, Dh)),
            "wq": _w((lambda *s: torch.randn(*s, dtype=torch.bfloat16) * 3.2)(D, Hq * Dh) * (D ** -0.5)),
            "wo": _w((lambda *s: torch.randn(*s, dtype=torch.bfloat16) * 0.8)(Hq * Dh, D) * ((Hq * Dh) ** -0.5)),
        }
    _A_in = _A_setup()

    def _B_setup():
        B, S, D, F = 2, 1, 512, 1792
        return {
            "x": _w((lambda *s: torch.randn(*s, dtype=torch.bfloat16) * 0.05)(B, S, D)),
            "w1": _w((lambda *s: torch.randn(*s, dtype=torch.bfloat16) * 3.2)(D, F) * (D ** -0.5)),
            "b1": _w((lambda *s: torch.randn(*s, dtype=torch.bfloat16) * 3.2)(F) * 0.01),
            "w2": _w((lambda *s: torch.randn(*s, dtype=torch.bfloat16) * 0.4)(F, D) * (F ** -0.5)),
            "b2": _w((lambda *s: torch.randn(*s, dtype=torch.bfloat16) * 0.4)(D) * 0.01),
        }
    _B_in = _B_setup()

    return {
        "A_x": _A_in["x"],
        "A_k_cache": _A_in["k_cache"],
        "A_v_cache": _A_in["v_cache"],
        "A_wq": _A_in["wq"],
        "A_wo": _A_in["wo"],
        "B_x": _B_in["x"],
        "B_w1": _B_in["w1"],
        "B_b1": _B_in["b1"],
        "B_w2": _B_in["w2"],
        "B_b2": _B_in["b2"],
    }

def reference(A_x, A_k_cache, A_v_cache, A_wq, A_wo, B_x, B_w1, B_b1, B_w2, B_b2):
    def _A_ref(x, k_cache, v_cache, wq, wo):
        B, Sq, D = x.shape
        Hq, Dh = 32, 128
        Hkv, Sk = k_cache.shape[1], k_cache.shape[2]
        W = min(4096, max(256, Sk // 4))
        rep = Hq // Hkv
        q = (lambda x, *d: x.permute(*d))((x @ wq).reshape(B, Sq, Hq, Dh), 0, 2, 1, 3)
        k = (lambda x, n, axis: torch.repeat_interleave(x, n, dim=axis))(k_cache[:, :, Sk - W:, :], rep, axis=1)
        v = (lambda x, n, axis: torch.repeat_interleave(x, n, dim=axis))(v_cache[:, :, Sk - W:, :], rep, axis=1)
        scores = torch.einsum("bhqd,bhkd->bhqk", q, k) * (Dh ** -0.5)
        p = _softmax(scores, axis=-1)
        o = torch.einsum("bhqk,bhkd->bhqd", p, v)
        return (lambda x, *d: x.permute(*d))(o, 0, 2, 1, 3).reshape(B, Sq, Hq * Dh) @ wo

    def _B_ref(x, w1, b1, w2, b2):
        h = torch.einsum("bsd,df->bsf", x, w1) + b1
        h = _gelu(h)
        return torch.einsum("bsf,fd->bsd", h, w2) + b2

    x = A_x
    h = x + _A_ref(_layernorm(x, axis=-1), A_k_cache, A_v_cache, A_wq, A_wo)
    h2 = h + _B_ref(_layernorm(h, axis=-1), B_w1, B_b1, B_w2, B_b2)
    return h2
```

```cpp
#include <hip/hip_runtime.h>
#include <hip/hip_bf16.h>
#include <stdint.h>

typedef unsigned short u16;
constexpr int B = 2, D = 512, HQ = 32, HKV = 8, SK = 8192, DH = 128, W = 2048, F = 1792;
constexpr int NSPLIT = 16, KPS = W / NSPLIT;

__device__ __forceinline__ float bf2f(u16 v) { return __uint_as_float(((unsigned)v) << 16); }
__device__ __forceinline__ u16 f2bf(float f) { __hip_bfloat16 h = __float2bfloat16(f); return *reinterpret_cast<u16*>(&h); }
__device__ __forceinline__ float rbf(float f) { return bf2f(f2bf(f)); }

__device__ void layernorm512(const float* in, float* out, float* red) {
  int tid = threadIdx.x;
  float a = in[tid], b = in[tid + 256];
  float s = a + b;
  for (int o = 32; o > 0; o >>= 1) s += __shfl_xor(s, o);
  if ((tid & 63) == 0) red[tid >> 6] = s;
  __syncthreads();
  float tot = red[0] + red[1] + red[2] + red[3];
  float mean = tot * (1.0f / 512.0f);
  __syncthreads();
  float da = a - mean, db = b - mean;
  float v = da * da + db * db;
  for (int o = 32; o > 0; o >>= 1) v += __shfl_xor(v, o);
  if ((tid & 63) == 0) red[tid >> 6] = v;
  __syncthreads();
  float var = (red[0] + red[1] + red[2] + red[3]) * (1.0f / 512.0f);
  __syncthreads();
  float mb = rbf(mean), vb = rbf(var);
  float den = rbf(sqrtf(rbf(vb + 1e-5f)));
  out[tid] = rbf(rbf(a - mb) / den);
  out[tid + 256] = rbf(rbf(b - mb) / den);
  __syncthreads();
}

__global__ void __launch_bounds__(256) k_qproj(const u16* x, const u16* wq, float* q) {
  __shared__ float xs[2][512], ln[2][512], red[4], part[4][2][64];
  int tid = threadIdx.x;
  for (int i = tid; i < 1024; i += 256) xs[i >> 9][i & 511] = bf2f(x[i]);
  __syncthreads();
  layernorm512(xs[0], ln[0], red);
  layernorm512(xs[1], ln[1], red);
  int col = blockIdx.x * 64 + (tid & 63), kg = tid >> 6;
  float a0 = 0, a1 = 0;
  for (int k = kg * 128; k < kg * 128 + 128; ++k) {
    float w = bf2f(wq[(size_t)k * 4096 + col]);
    a0 += ln[0][k] * w; a1 += ln[1][k] * w;
  }
  part[kg][0][tid & 63] = a0; part[kg][1][tid & 63] = a1;
  __syncthreads();
  if (tid < 128) {
    int b = tid >> 6, c = tid & 63;
    float s = part[0][b][c] + part[1][b][c] + part[2][b][c] + part[3][b][c];
    q[b * 4096 + blockIdx.x * 64 + c] = rbf(s);
  }
}

__global__ void __launch_bounds__(256) k_attn(const float* q, const u16* kc, const u16* vc, float* po, float* pml) {
  __shared__ float qs[4][128], sc[4][KPS], ml[4][2];
  int s = blockIdx.x, kvh = blockIdx.y, b = blockIdx.z, tid = threadIdx.x;
  for (int i = tid; i < 512; i += 256) qs[i >> 7][i & 127] = q[b * 4096 + kvh * 512 + i];
  __syncthreads();
  size_t base = ((size_t)(b * HKV + kvh) * SK + (SK - W) + s * KPS) * DH;
  for (int i = tid; i < 4 * KPS; i += 256) {
    int h = i / KPS, key = i % KPS;
    const u16* kr = kc + base + (size_t)key * DH;
    float a = 0;
    for (int d = 0; d < 128; ++d) a += qs[h][d] * bf2f(kr[d]);
    sc[h][key] = rbf(rbf(a) * 0.08838834764831845f);
  }
  __syncthreads();
  int wv = tid >> 6, lane = tid & 63;
  {
    float a = sc[wv][lane], c = sc[wv][lane + 64];
    float m = fmaxf(a, c);
    for (int o = 32; o > 0; o >>= 1) m = fmaxf(m, __shfl_xor(m, o));
    float ea = expf(a - m), ec = expf(c - m);
    float l = ea + ec;
    for (int o = 32; o > 0; o >>= 1) l += __shfl_xor(l, o);
    sc[wv][lane] = ea; sc[wv][lane + 64] = ec;
    if (lane == 0) { ml[wv][0] = m; ml[wv][1] = l; }
  }
  __syncthreads();
  for (int i = tid; i < 512; i += 256) {
    int h = i >> 7, d = i & 127;
    float a = 0;
    for (int key = 0; key < KPS; ++key) a += sc[h][key] * bf2f(vc[base + (size_t)key * DH + d]);
    po[((size_t)(b * HQ + kvh * 4 + h) * NSPLIT + s) * DH + d] = a;
  }
  if (tid < 8) pml[((b * HQ + kvh * 4 + (tid >> 1)) * NSPLIT + s) * 2 + (tid & 1)] = ml[tid >> 1][tid & 1];
}

__global__ void __launch_bounds__(256) k_oproj(const float* po, const float* pml, const u16* wo, float* part) {
  __shared__ float o[2][128];
  int h = blockIdx.x, tid = threadIdx.x;
  {
    int b = tid >> 7, d = tid & 127;
    const float* ml = pml + (b * HQ + h) * NSPLIT * 2;
    float M = -INFINITY;
    for (int s = 0; s < NSPLIT; ++s) M = fmaxf(M, ml[2 * s]);
    float L = 0, a = 0;
    for (int s = 0; s < NSPLIT; ++s) {
      float w = expf(ml[2 * s] - M);
      L += w * ml[2 * s + 1];
      a += w * po[((size_t)(b * HQ + h) * NSPLIT + s) * DH + d];
    }
    o[b][d] = rbf(a / L);
  }
  __syncthreads();
  for (int c = tid; c < 512; c += 256) {
    float a0 = 0, a1 = 0;
    for (int r = 0; r < 128; ++r) {
      float w = bf2f(wo[(size_t)(h * 128 + r) * 512 + c]);
      a0 += o[0][r] * w; a1 += o[1][r] * w;
    }
    part[(h * 2 + 0) * 512 + c] = a0; part[(h * 2 + 1) * 512 + c] = a1;
  }
}

__device__ __forceinline__ float gelu_tanh(float x) {
  const float k0 = 0.7978845608028654f, k1 = 0.044715f;
  float inner = k0 * (x + k1 * x * x * x);
  return 0.5f * x * (1.0f + tanhf(inner));
}

__global__ void __launch_bounds__(256) k_ffn(const u16* x, const float* part, const u16* w1, const u16* b1, const u16* w2, float* fpart, float* hout) {
  __shared__ float hs[2][512], ln[2][512], red[4], p1[4][2][64], g[2][64];
  int tid = threadIdx.x;
  for (int i = tid; i < 1024; i += 256) {
    int b = i >> 9, c = i & 511;
    float s = 0;
    for (int h = 0; h < 32; ++h) s += part[(h * 2 + b) * 512 + c];
    float hv = rbf(bf2f(x[i]) + rbf(s));
    hs[b][c] = hv;
    if (blockIdx.x == 0) hout[i] = hv;
  }
  __syncthreads();
  layernorm512(hs[0], ln[0], red);
  layernorm512(hs[1], ln[1], red);
  int col = blockIdx.x * 64 + (tid & 63), kg = tid >> 6;
  float a0 = 0, a1 = 0;
  for (int k = kg * 128; k < kg * 128 + 128; ++k) {
    float w = bf2f(w1[(size_t)k * F + col]);
    a0 += ln[0][k] * w; a1 += ln[1][k] * w;
  }
  p1[kg][0][tid & 63] = a0; p1[kg][1][tid & 63] = a1;
  __syncthreads();
  if (tid < 128) {
    int b = tid >> 6, c = tid & 63;
    float s = p1[0][b][c] + p1[1][b][c] + p1[2][b][c] + p1[3][b][c];
    float z = rbf(rbf(s) + bf2f(b1[blockIdx.x * 64 + c]));
    g[b][c] = rbf(gelu_tanh(z));
  }
  __syncthreads();
  for (int c = tid; c < 512; c += 256) {
    float s0 = 0, s1 = 0;
    for (int r = 0; r < 64; ++r) {
      float w = bf2f(w2[(size_t)(blockIdx.x * 64 + r) * 512 + c]);
      s0 += g[0][r] * w; s1 += g[1][r] * w;
    }
    fpart[(blockIdx.x * 2 + 0) * 512 + c] = s0; fpart[(blockIdx.x * 2 + 1) * 512 + c] = s1;
  }
}

__global__ void __launch_bounds__(256) k_final(const float* fpart, const float* hin, const u16* b2, u16* out) {
  int i = blockIdx.x * 256 + threadIdx.x;
  int b = i >> 9, c = i & 511;
  float s = 0;
  for (int j = 0; j < 28; ++j) s += fpart[(j * 2 + b) * 512 + c];
  float y = rbf(rbf(s) + bf2f(b2[c]));
  out[i] = f2bf(hin[i] + y);
}

extern "C" void kernel_launch(void* const* d_in, const int* in_sizes, int n_in,
                              void* d_out, int out_size, void* d_ws, size_t ws_size,
                              hipStream_t stream) {
  const u16* x = (const u16*)d_in[0];
  const u16* kc = (const u16*)d_in[1];
  const u16* vc = (const u16*)d_in[2];
  const u16* wq = (const u16*)d_in[3];
  const u16* wo = (const u16*)d_in[4];
  const u16* w1 = (const u16*)d_in[6];
  const u16* b1 = (const u16*)d_in[7];
  const u16* w2 = (const u16*)d_in[8];
  const u16* b2 = (const u16*)d_in[9];
  float* ws = (float*)d_ws;
  float* q = ws;
  float* po = q + 8192;
  float* pml = po + 131072;
  float* part = pml + 2048;
  float* fpart = part + 32768;
  float* hbuf = fpart + 28 * 1024;
  k_qproj<<<64, 256, 0, stream>>>(x, wq, q);
  k_attn<<<dim3(NSPLIT, HKV, B), 256, 0, stream>>>(q, kc, vc, po, pml);
  k_oproj<<<32, 256, 0, stream>>>(po, pml, wo, part);
  k_ffn<<<28, 256, 0, stream>>>(x, part, w1, b1, w2, fpart, hbuf);
  k_final<<<4, 256, 0, stream>>>(fpart, hbuf, b2, (u16*)d_out);
}
```

```cpp
#include <hip/hip_runtime.h>
#include <hip/hip_bf16.h>
#include <stdint.h>

typedef unsigned short u16;
typedef unsigned long long u64;
typedef unsigned v4u __attribute__((ext_vector_type(4)));
typedef unsigned v2u __attribute__((ext_vector_type(2)));
typedef float f32x4 __attribute__((ext_vector_type(4)));
typedef float f32x2 __attribute__((ext_vector_type(2)));
typedef short bf16x8 __attribute__((ext_vector_type(8)));
typedef short bf16x4 __attribute__((ext_vector_type(4)));
typedef __bf16 bf16x2 __attribute__((ext_vector_type(2)));
typedef short v4i16 __attribute__((ext_vector_type(4)));
#define LAS __attribute__((address_space(3)))
#define GAS __attribute__((address_space(1)))

namespace {

constexpr int SK = 8192, WIN = 2048, FF = 1792;
constexpr int NTHR = 576;
constexpr int CODE_KB = 12;
constexpr unsigned SPIN_CAP = 1u << 18;
constexpr float LOG2E = 1.4426950408889634f;

constexpr unsigned WS_EPOCH = 0;
constexpr unsigned WS_QG = 4096;
constexpr unsigned WS_PA = WS_QG + 8 * 4 * 2 * 512 * 8;
constexpr unsigned WS_PB = WS_PA + 2 * 32 * 16 * 640;
constexpr unsigned WS_HG = WS_PB + 32 * 32 * 256;
constexpr unsigned WS_PD = WS_HG + 32 * 128;
constexpr unsigned WS_END = WS_PD + 64 * 56 * 128;
constexpr unsigned WS_STAMP = 4u << 20;

constexpr int L_WQ = 0;
constexpr int RED_PITCH = 2080;
constexpr int L_RED = 0;
constexpr int L_W = 17408;
constexpr int L_ACT = L_W + 65536;
constexpr int ACT_PITCH = 256 + 64;
constexpr int L_QS = L_ACT + 2 * ACT_PITCH;
constexpr int QS_PITCH = 256 + 64;
constexpr int L_MX = L_QS + 4 * QS_PITCH;
constexpr int L_LS = L_MX + 128;
constexpr int L_WN = L_LS + 128;
constexpr int L_ZP = L_WN + 128;
constexpr int L_GA = L_ZP + 64 * 48;
constexpr int L_TAG = L_GA + 8 * 160;
constexpr int L_PF = L_TAG + 64;
constexpr int LDS_BYTES = L_PF + 1024;
static_assert(8 * RED_PITCH <= L_W, "lds map");
static_assert(LDS_BYTES > 81920, "one workgroup per CU");

struct Params {
  const u16* x; const u16* kc; const u16* vc; const u16* wq; const u16* wo;
  const u16* w1; const u16* b1; const u16* w2; const u16* b2;
  u16* out; unsigned char* ws;
};

__device__ __forceinline__ float bf_lo(unsigned d) { return __uint_as_float(d << 16); }
__device__ __forceinline__ float bf_hi(unsigned d) { return __uint_as_float(d & 0xffff0000u); }
__device__ __forceinline__ unsigned pk_bf16(float lo, float hi) {
  return __builtin_bit_cast(unsigned, __builtin_convertvector(f32x2{lo, hi}, bf16x2));
}
__device__ __forceinline__ float rbf(float f) { return bf_lo(pk_bf16(f, f)); }
__device__ __forceinline__ f32x4 mfma(v4u a, v4u b, f32x4 c) {
  return __builtin_amdgcn_mfma_f32_16x16x32_bf16(__builtin_bit_cast(bf16x8, a), __builtin_bit_cast(bf16x8, b), c, 0, 0, 0);
}
__device__ __forceinline__ f32x4 mfma16(v2u a, v2u b, f32x4 c) {
  return __builtin_amdgcn_mfma_f32_16x16x16bf16_1k(__builtin_bit_cast(bf16x4, a), __builtin_bit_cast(bf16x4, b), c, 0, 0, 0);
}
__device__ __forceinline__ float dot2(unsigned a, unsigned b, float acc) {
  return __builtin_amdgcn_fdot2_f32_bf16(__builtin_bit_cast(bf16x2, a), __builtin_bit_cast(bf16x2, b), acc, false);
}
template <int CTRL> __device__ __forceinline__ float dpp(float x) {
  return __builtin_bit_cast(float, __builtin_amdgcn_mov_dpp(__builtin_bit_cast(int, x), CTRL, 0xf, 0xf, true));
}
constexpr int XOR1 = 0xB1, XOR2 = 0x4E, HMIR = 0x141, ROR8 = 0x128, ROR4 = 0x124;
__device__ __forceinline__ float x16_sum(float x) {
  auto s = __builtin_amdgcn_permlane16_swap(__float_as_uint(x), __float_as_uint(x), false, false);
  return __uint_as_float(s[0]) + __uint_as_float(s[1]);
}
__device__ __forceinline__ float x32_sum(float x) {
  auto s = __builtin_amdgcn_permlane32_swap(__float_as_uint(x), __float_as_uint(x), false, false);
  return __uint_as_float(s[0]) + __uint_as_float(s[1]);
}
__device__ __forceinline__ float x16_max(float x) {
  auto s = __builtin_amdgcn_permlane16_swap(__float_as_uint(x), __float_as_uint(x), false, false);
  return fmaxf(__uint_as_float(s[0]), __uint_as_float(s[1]));
}
__device__ __forceinline__ float x32_max(float x) {
  auto s = __builtin_amdgcn_permlane32_swap(__float_as_uint(x), __float_as_uint(x), false, false);
  return fmaxf(__uint_as_float(s[0]), __uint_as_float(s[1]));
}
__device__ __forceinline__ float wave_sum(float x) {
  x += dpp<XOR1>(x); x += dpp<XOR2>(x); x += dpp<HMIR>(x); x += dpp<ROR8>(x);
  return x32_sum(x16_sum(x));
}
__device__ __forceinline__ float wave_sum_par(float x) {
  x += dpp<XOR2>(x); x += dpp<ROR4>(x); x += dpp<ROR8>(x);
  return x32_sum(x16_sum(x));
}
__device__ __forceinline__ float max8(f32x4 a, f32x4 b) {
  return fmaxf(fmaxf(fmaxf(a[0], a[1]), fmaxf(a[2], a[3])), fmaxf(fmaxf(b[0], b[1]), fmaxf(b[2], b[3])));
}
#define BAR() asm volatile("s_waitcnt lgkmcnt(0)\n\ts_barrier" ::: "memory")
#define RBAR() asm volatile("s_barrier" ::: "memory")
#define CBAR() asm volatile("" ::: "memory")
#define LDNT(p) __builtin_nontemporal_load((const v4u*)(p))
#define SC1 16

__device__ __forceinline__ void dma16(const void* sbase, unsigned voff, unsigned ldsaddr) {
  asm volatile("s_mov_b32 m0, %2\n\ts_nop 0\n\tglobal_load_lds_dwordx4 %0, %1 nt" ::"v"(voff), "s"(sbase), "s"(ldsaddr) : "memory");
}
__device__ __forceinline__ v4u tr_frag(LAS unsigned char* p0, LAS unsigned char* p1) {
  v2u a = __builtin_bit_cast(v2u, __builtin_amdgcn_ds_read_tr16_b64_v4i16((LAS v4i16*)p0));
  v2u b = __builtin_bit_cast(v2u, __builtin_amdgcn_ds_read_tr16_b64_v4i16((LAS v4i16*)p1));
  return v4u{a.x, a.y, b.x, b.y};
}
__device__ __forceinline__ v4u ldg(__amdgpu_buffer_rsrc_t rs, unsigned off) { return __builtin_amdgcn_raw_buffer_load_b128(rs, off, 0, SC1); }

__device__ __forceinline__ v4u ln_row(v4u d) {
  float v[8];
  float s = 0.f;
#pragma unroll
  for (int i = 0; i < 4; ++i) { v[2 * i] = bf_lo(d[i]); v[2 * i + 1] = bf_hi(d[i]); s += v[2 * i] + v[2 * i + 1]; }
  const float mean = wave_sum(s) * (1.0f / 512.0f);
  float q = 0.f;
#pragma unroll
  for (int i = 0; i < 8; ++i) { const float t = v[i] - mean; q = fmaf(t, t, q); }
  q = wave_sum(q);
  const float mb = rbf(mean), vb = rbf(q * (1.0f / 512.0f));
  const float inv = __builtin_amdgcn_rcpf(rbf(__builtin_amdgcn_sqrtf(rbf(vb + 1e-5f))));
  v4u r;
#pragma unroll
  for (int i = 0; i < 4; ++i) {
    const unsigned c = pk_bf16(v[2 * i] - mb, v[2 * i + 1] - mb);
    r[i] = pk_bf16(bf_lo(c) * inv, bf_hi(c) * inv);
  }
  return r;
}

#define STAMP(i) do {} while (0)
#define STAMP_DECL
#define STAMP_FLUSH() do {} while (0)

__global__ void __launch_bounds__(NTHR, 1) fused_decode(Params p) {
  extern __shared__ __attribute__((aligned(16))) unsigned char lds_raw[];
  u64 pc;
  asm volatile("s_getpc_b64 %0" : "=s"(pc));
  LAS unsigned char* L = (LAS unsigned char*)lds_raw;
  const unsigned LB = (unsigned)(size_t)L;
  const int tid = threadIdx.x, lane = tid & 63, wave = __builtin_amdgcn_readfirstlane(tid >> 6);
  const int kvh = blockIdx.x & 7, j = blockIdx.x >> 3;
  const int g = lane >> 4, c = lane & 15;
  const __amdgpu_buffer_rsrc_t rs = __builtin_amdgcn_make_buffer_rsrc((void*)p.ws, 0, WS_END, 0x00020000);
  STAMP_DECL;
  STAMP(0);
  unsigned tag;

  if (wave == 0) {
    const int rg = j >> 3, cg = j & 7, b = j >> 4, sp = j & 15;
    unsigned ep; v4u x0, x1;
    asm volatile("global_load_dword %0, %1, %2 sc1" : "=v"(ep) : "v"(WS_EPOCH), "s"(p.ws) : "memory");
    asm volatile("global_load_dwordx4 %0, %1, %2" : "=v"(x0) : "v"(lane * 16), "s"(p.x) : "memory");
    asm volatile("global_load_dwordx4 %0, %1, %2 offset:1024" : "=v"(x1) : "v"(lane * 16), "s"(p.x) : "memory");
    {
      const int r0 = lane >> 3, pos = lane & 7;
      const unsigned v0 = r0 * 8192 + (((pos >> 1) ^ ((r0 >> 1) & 1)) * 32) + (pos & 1) * 16;
      const u16* src = p.wq + (size_t)(rg * 128) * 4096 + kvh * 512 + cg * 64;
#pragma unroll
      for (int i = 0; i < 16; ++i) dma16(src + (size_t)i * 8 * 4096, (i & 1) ? (v0 ^ 64u) : v0, LB + L_WQ + i * 1024);
    }
    RBAR();
    RBAR();
    asm volatile("s_waitcnt vmcnt(16)" : "+v"(ep), "+v"(x0), "+v"(x1)::"memory");
    tag = __builtin_amdgcn_readfirstlane(ep) + 1u;
    if (lane == 0) *(LAS unsigned*)(L + L_TAG) = tag;
    STAMP(1);
    {
      const v4u a = ln_row(x0), bb = ln_row(x1);
      if (g == rg) {
        *(LAS v4u*)(L + L_ACT + c * 16) = a;
        *(LAS v4u*)(L + L_ACT + ACT_PITCH + c * 16) = bb;
      }
    }
    CBAR();
    v4u af[4];
#pragma unroll
    for (int s = 0; s < 4; ++s) af[s] = *(LAS v4u*)(L + L_ACT + (c & 1) * ACT_PITCH + s * 64 + g * 16);
    f32x4 acc[4] = {};
    {
      const int q4 = (lane >> 2) & 3, pp = lane & 3;
      const int f = ((q4 >> 1) & 1) | ((g & 1) << 1);
      LAS unsigned char* base = L + L_WQ + (8 * g + q4) * 128 + pp * 8;
#pragma unroll
      for (int s = 0; s < 4; ++s) {
        if (s == 0) asm volatile("s_waitcnt vmcnt(12)" ::: "memory");
        if (s == 1) asm volatile("s_waitcnt vmcnt(8)" ::: "memory");
        if (s == 2) asm volatile("s_waitcnt vmcnt(4)" ::: "memory");
        if (s == 3) { asm volatile("s_waitcnt vmcnt(0)" ::: "memory"); STAMP(2); }
#pragma unroll
        for (int t = 0; t < 4; ++t) {
          LAS unsigned char* q = base + s * 4096 + ((t ^ f) * 32);
          acc[t] = mfma(af[s], tr_frag(q, q + 512), acc[t]);
        }
      }
    }
    {
      const float s0 = (g & 1) ? acc[1][0] : acc[0][0], s1 = (g & 1) ? acc[1][1] : acc[0][1];
      const float s2 = (g & 1) ? acc[3][0] : acc[2][0], s3 = (g & 1) ? acc[3][1] : acc[2][1];
      const float q0 = (g & 2) ? s2 : s0, q1 = (g & 2) ? s3 : s1;
      const unsigned off = WS_QG + (((kvh * 4 + rg) * 2) * 512 + cg * 64 + lane) * 8;
      __builtin_amdgcn_raw_buffer_store_b64(v2u{__float_as_uint(q0), tag}, rs, off, 0, SC1);
      __builtin_amdgcn_raw_buffer_store_b64(v2u{__float_as_uint(q1), tag}, rs, off + 4096, 0, SC1);
    }
    STAMP(3);
    RBAR();
    {
      const unsigned off = WS_QG + ((kvh * 4 * 2 + b) * 512 + 2 * lane) * 8;
      v4u d[16];
#pragma unroll
      for (int i = 0; i < 16; ++i) d[i] = ldg(rs, off + (i >> 2) * 8192 + (i & 3) * 1024);
      RBAR();
      for (unsigned spin = 0; spin < SPIN_CAP; ++spin) {
        bool ok = true;
#pragma unroll
        for (int i = 0; i < 16; ++i) ok &= (d[i].y == tag) & (d[i].w == tag);
        if (__all(ok)) break;
        CBAR();
#pragma unroll
        for (int i = 0; i < 16; ++i) d[i] = ldg(rs, off + (i >> 2) * 8192 + (i & 3) * 1024);
      }
#pragma unroll
      for (int hh = 0; hh < 4; ++hh) {
        const float lo = (__uint_as_float(d[hh].x) + __uint_as_float(d[4 + hh].x)) + (__uint_as_float(d[8 + hh].x) + __uint_as_float(d[12 + hh].x));
        const float hi = (__uint_as_float(d[hh].z) + __uint_as_float(d[4 + hh].z)) + (__uint_as_float(d[8 + hh].z) + __uint_as_float(d[12 + hh].z));
        *(LAS unsigned*)(L + L_QS + hh * QS_PITCH + lane * 4) = pk_bf16(lo, hi);
      }
    }
    STAMP(4);
    BAR();
    RBAR();
    {
      const float mw = *(LAS float*)(L + L_MX + g * 32 + (c & 7) * 4), lw = *(LAS float*)(L + L_LS + g * 32 + (c & 7) * 4);
      float M = fmaxf(mw, dpp<XOR1>(mw)); M = fmaxf(M, dpp<XOR2>(M)); M = fmaxf(M, dpp<HMIR>(M));
      float S = lw * __builtin_amdgcn_exp2f(mw - M);
      S += dpp<XOR1>(S); S += dpp<XOR2>(S); S += dpp<HMIR>(S);
      if (c == 0)
        __builtin_amdgcn_raw_buffer_store_b128(v4u{__float_as_uint(M), tag, __float_as_uint(S), tag}, rs,
                                               WS_PA + ((b * 32 + kvh * 4 + g) * 16 + sp) * 640 + 512, 0, SC1);
    }
    if (j < 16) {
      const int h = kvh * 4 + (j >> 2), cq = j & 3;
      const unsigned v0 = g * 1024 + (((c >> 1) ^ g) * 32) + (c & 1) * 16;
      const u16* src = p.wo + (size_t)(h * 128) * 512 + cq * 128;
#pragma unroll
      for (int i = 0; i < 32; ++i) dma16(src + (size_t)i * 4 * 512, (i & 2) ? (v0 ^ 128u) : v0, LB + L_W + i * 1024);
    } else if (j < 23) {
      const int f = kvh * 7 + (j - 16);
      const int r0 = lane >> 2, pos = lane & 3;
      const unsigned v0 = r0 * (FF * 2) + (((pos >> 1) ^ ((r0 >> 3) & 1)) * 32) + (pos & 1) * 16;
      const u16* src = p.w1 + f * 32;
#pragma unroll
      for (int i = 0; i < 32; ++i) dma16(src + (size_t)i * 16 * FF, v0, LB + L_W + i * 1024);
    }
    RBAR();
    if (j >= 16 && j < 23) {
      const int f = kvh * 7 + (j - 16);
      const u16* src = p.w2 + (size_t)(f * 32) * 512;
#pragma unroll
      for (int i = 0; i < 32; ++i)
        dma16(src + (size_t)i * 512, (unsigned)(lane * 16) ^ (unsigned)(((i & 3) | (((i >> 3) & 1) << 2)) * 32), LB + L_W + 32768 + i * 1024);
    }
  } else {
    const int w = wave - 1, b = j >> 4, sp = j & 15;
    if (w == 7 && j < CODE_KB) {
      asm volatile("s_mov_b32 m0, %2\n\ts_nop 0\n\tglobal_load_lds_dwordx4 %0, %1" ::"v"(j * 1024 + lane * 16), "s"(pc & ~255ull), "s"(LB + L_PF) : "memory");
    }
    const size_t base = ((size_t)(b * 8 + kvh) * SK + (SK - WIN) + sp * 128 + w * 16) * 128;
    const u16* kp = p.kc + base + c * 128 + g * 8;
    const u16* vp = p.vc + base + (4 * g) * 128 + c * 8;
    v4u kf[4], vf[4];
    RBAR();
#pragma unroll
    for (int s = 0; s < 4; ++s) kf[s] = LDNT(kp + s * 32);
    RBAR();
    vf[0] = LDNT(vp); vf[1] = LDNT(vp + 128); vf[2] = LDNT(vp + 256);
    asm volatile("" : "+v"(kf[0]), "+v"(kf[1]), "+v"(kf[2]), "+v"(kf[3])::"memory");
    STAMP(1);
    RBAR();
    RBAR();
    vf[3] = LDNT(vp + 384);
    const unsigned mk0 = (c & 2) ? 0u : ((c & 1) ? 0xffff0000u : 0x0000ffffu), mk1 = (c & 2) ? ((c & 1) ? 0xffff0000u : 0x0000ffffu) : 0u;
    RBAR();
    STAMP(2);
    tag = *(LAS unsigned*)(L + L_TAG);
    f32x4 sc = {};
#pragma unroll
    for (int s = 0; s < 4; ++s) sc = mfma(kf[s], *(LAS v4u*)(L + L_QS + (c >> 2) * QS_PITCH + s * 64 + g * 16), sc);
    float sv[4];
    {
      const unsigned r01 = pk_bf16(sc[0], sc[1]), r23 = pk_bf16(sc[2], sc[3]);
      const float k = 0.08838834764831845f;
      const unsigned t01 = pk_bf16(bf_lo(r01) * k, bf_hi(r01) * k), t23 = pk_bf16(bf_lo(r23) * k, bf_hi(r23) * k);
      sv[0] = bf_lo(t01); sv[1] = bf_hi(t01); sv[2] = bf_lo(t23); sv[3] = bf_hi(t23);
    }
    const float mw = x32_max(x16_max(fmaxf(fmaxf(sv[0], sv[1]), fmaxf(sv[2], sv[3]))));
    v2u pa[4];
    float lw = 0.f;
#pragma unroll
    for (int t = 0; t < 4; ++t) {
      const float e = __builtin_amdgcn_exp2f((sv[t] - mw) * LOG2E);
      lw += e;
      const unsigned pk = pk_bf16(e, e);
      pa[t] = v2u{pk & mk0, pk & mk1};
    }
    lw = x32_sum(x16_sum(lw));
    if ((lane & 51) == 0) {
      *(LAS float*)(L + L_MX + (c >> 2) * 32 + w * 4) = mw * LOG2E;
      *(LAS float*)(L + L_LS + (c >> 2) * 32 + w * 4) = lw;
    }
    STAMP(3);
    BAR();
    const float fac = __builtin_amdgcn_exp2f(*(LAS float*)(L + L_MX + g * 32 + w * 4) -
                                             max8(*(LAS f32x4*)(L + L_MX + g * 32), *(LAS f32x4*)(L + L_MX + g * 32 + 16)));
    f32x4 olo = {}, ohi = {};
#pragma unroll
    for (int t = 0; t < 4; ++t) { olo = mfma16(pa[t], v2u{vf[t].x, vf[t].y}, olo); ohi = mfma16(pa[t], v2u{vf[t].z, vf[t].w}, ohi); }
    *(LAS f32x4*)(L + L_RED + w * RED_PITCH + g * 512 + c * 16) = olo * fac;
    *(LAS f32x4*)(L + L_RED + w * RED_PITCH + g * 512 + 256 + c * 16) = ohi * fac;
    STAMP(4);
    BAR();
    {
      const int hh = w >> 1, pr = (w & 1) * 32 + (lane >> 1), half = lane & 1;
      LAS unsigned char* src = L + L_RED + (4 * half) * RED_PITCH + hh * 512 + ((pr >> 1) & 1) * 256 + (pr >> 2) * 16 + (pr & 1) * 8;
      f32x2 a = (*(LAS f32x2*)(src) + *(LAS f32x2*)(src + RED_PITCH)) + (*(LAS f32x2*)(src + 2 * RED_PITCH) + *(LAS f32x2*)(src + 3 * RED_PITCH));
      a[0] += dpp<XOR1>(a[0]); a[1] += dpp<XOR1>(a[1]);
      if (half == 0)
        __builtin_amdgcn_raw_buffer_store_b64(v2u{pk_bf16(a[0], a[1]), tag}, rs, WS_PA + ((b * 32 + kvh * 4 + hh) * 16 + sp) * 640 + pr * 8, 0, SC1);
    }
    STAMP(5);
  }

  if (j < 16) {
    const int h = kvh * 4 + (j >> 2), cq = j & 3;
    if (wave == 0) {
      const unsigned off = WS_PA + (((g & 1) * 32 + h) * 16 + c) * 640 + 512;
      v4u d;
      for (unsigned spin = 0; spin < SPIN_CAP; ++spin) {
        CBAR();
        d = ldg(rs, off);
        if (__all((d.y == tag) & (d.w == tag))) break;
      }
      const float m = __uint_as_float(d.x);
      float M = fmaxf(m, dpp<XOR1>(m)); M = fmaxf(M, dpp<XOR2>(M)); M = fmaxf(M, dpp<HMIR>(M)); M = fmaxf(M, dpp<ROR8>(M));
      const float wv = __builtin_amdgcn_exp2f(m - M);
      float S = wv * __uint_as_float(d.z);
      S += dpp<XOR1>(S); S += dpp<XOR2>(S); S += dpp<HMIR>(S); S += dpp<ROR8>(S);
      if (g < 2) *(LAS float*)(L + L_WN + lane * 4) = wv / S;
      asm volatile("s_waitcnt vmcnt(0)" ::: "memory");
      STAMP(6);
      BAR();
      STAMP_FLUSH();
      return;
    }
    const int w = wave - 1;
    const int b = w >> 2, dq = w & 3, s8 = lane & 7, xx = lane >> 3;
    const unsigned off = WS_PA + ((b * 32 + h) * 16 + s8) * 640 + dq * 128 + xx * 16;
    v4u d0 = ldg(rs, off), d1 = ldg(rs, off + 8 * 640);
    RBAR();
    v4u wf[4];
    {
      const int q4 = (lane >> 2) & 3, pp = lane & 3;
      const int f = q4 | ((g & 1) << 2);
#pragma unroll
      for (int s = 0; s < 4; ++s) {
        LAS unsigned char* base = L + L_W + (32 * s + 8 * g + q4) * 256 + ((w ^ f) * 32) + pp * 8;
        wf[s] = tr_frag(base, base + 1024);
      }
    }
    float w0 = *(LAS float*)(L + L_WN + b * 64 + s8 * 4), w1 = *(LAS float*)(L + L_WN + b * 64 + 32 + s8 * 4);
    asm volatile("" : "+v"(wf[0]), "+v"(wf[1]), "+v"(wf[2]), "+v"(wf[3]), "+v"(w0), "+v"(w1));
    for (unsigned spin = 0; spin < SPIN_CAP; ++spin) {
      if (__all((d0.y == tag) & (d0.w == tag) & (d1.y == tag) & (d1.w == tag))) break;
      CBAR();
      d0 = ldg(rs, off); d1 = ldg(rs, off + 8 * 640);
    }
    STAMP(6);
    {
      const float v0 = w0 * bf_lo(d0.x) + w1 * bf_lo(d1.x), v1 = w0 * bf_hi(d0.x) + w1 * bf_hi(d1.x);
      const float v2 = w0 * bf_lo(d0.z) + w1 * bf_lo(d1.z), v3 = w0 * bf_hi(d0.z) + w1 * bf_hi(d1.z);
      const float a0 = v0 + dpp<HMIR>(v0), a1 = v1 + dpp<HMIR>(v1), a2 = v2 + dpp<HMIR>(v2), a3 = v3 + dpp<HMIR>(v3);
      float u0 = (s8 & 4) ? a2 : a0, u1 = (s8 & 4) ? a3 : a1;
      u0 += dpp<XOR2>(u0); u1 += dpp<XOR2>(u1);
      u0 += dpp<XOR1>(u0); u1 += dpp<XOR1>(u1);
      if ((s8 & 3) == 0) *(LAS unsigned*)(L + L_ACT + b * ACT_PITCH + (dq * 32 + xx * 4 + (s8 >> 2) * 2) * 2) = pk_bf16(u0, u1);
    }
    BAR();
    f32x4 acc = {};
#pragma unroll
    for (int s = 0; s < 4; ++s) acc = mfma(*(LAS v4u*)(L + L_ACT + (c & 1) * ACT_PITCH + s * 64 + g * 16), wf[s], acc);
    if (g == 0)
      __builtin_amdgcn_raw_buffer_store_b128(v4u{__float_as_uint(acc[0]), tag, __float_as_uint(acc[1]), tag}, rs,
                                             WS_PB + ((cq * 8 + w) * 32 + h) * 256 + c * 16, 0, SC1);
    STAMP(7);
  } else if (j < 23) {
    const int f = kvh * 7 + (j - 16);
    if (wave == 0) {
      asm volatile("s_waitcnt vmcnt(0)" ::: "memory");
      STAMP(6);
      RBAR();
      RBAR();
      RBAR();
      const unsigned off = WS_HG + lane * 64;
      v4u d[4];
      for (unsigned spin = 0; spin < SPIN_CAP; ++spin) {
        CBAR();
        bool ok = true;
#pragma unroll
        for (int i = 0; i < 4; ++i) d[i] = ldg(rs, off + i * 16);
#pragma unroll
        for (int i = 0; i < 4; ++i) ok &= (d[i].y == tag) & (d[i].w == tag);
        if (__all(ok)) break;
      }
      STAMP(7);
      float s0 = 0.f, s1 = 0.f, q0 = 0.f, q1 = 0.f;
#pragma unroll
      for (int i = 0; i < 4; ++i) {
        s0 = dot2(d[i].x, 0x3f803f80u, s0); s1 = dot2(d[i].z, 0x3f803f80u, s1);
        q0 = dot2(d[i].x, d[i].x, q0); q1 = dot2(d[i].z, d[i].z, q1);
      }
      const float mean = wave_sum_par(s0 + s1) * (1.0f / 512.0f);
      const float ex2 = wave_sum_par(q0 + q1) * (1.0f / 512.0f);
      const float var = fmaxf(fmaf(-mean, mean, ex2), 0.f);
      const float inv = __builtin_amdgcn_rcpf(rbf(__builtin_amdgcn_sqrtf(rbf(rbf(var) + 1e-5f))));
      if (lane < 2) *(LAS f32x2*)(L + L_WN + lane * 8) = f32x2{rbf(mean), inv};
      BAR();
      STAMP(8);
      STAMP_FLUSH();
      return;
    }
    const int w = wave - 1;
    const float bias1 = bf_lo(p.b1[f * 32 + (lane & 31)]);
    RBAR();
    v4u w1f[4], w2f[4];
    {
      const int q4 = (lane >> 2) & 3, pp = lane & 3;
#pragma unroll
      for (int s = 0; s < 4; ++s) {
        LAS unsigned char* base = L + L_W + (64 * w + 32 * (s >> 1) + 8 * g + q4) * 64 + (((s & 1) ^ (g & 1)) * 32) + pp * 8;
        w1f[s] = tr_frag(base, base + 256);
      }
      const int fz = q4 | ((g & 1) << 2);
#pragma unroll
      for (int t = 0; t < 4; ++t) {
        LAS unsigned char* base = L + L_W + 32768 + (8 * g + q4) * 1024 + (((4 * w + t) ^ fz) * 32) + pp * 8;
        w2f[t] = tr_frag(base, base + 4096);
      }
    }
    const f32x4 z4 = {};
    LAS float* zdst = (LAS float*)(L + L_ZP + ((g & 1) * 32 + (g >> 1) * 16 + c) * 48 + w * 4);
    float csum;
    {
      const v4u ones = {0x3f803f80u, 0x3f803f80u, 0x3f803f80u, 0x3f803f80u};
      const f32x4 c0 = mfma(ones, w1f[2], mfma(ones, w1f[0], z4)), c1 = mfma(ones, w1f[3], mfma(ones, w1f[1], z4));
      *zdst = (g & 2) ? c1[0] : c0[0];
      BAR();
      const f32x4 a = *(LAS f32x4*)(L + L_ZP + lane * 48), bq = *(LAS f32x4*)(L + L_ZP + lane * 48 + 16);
      csum = ((a[0] + a[1]) + (a[2] + a[3])) + ((bq[0] + bq[1]) + (bq[2] + bq[3]));
      asm volatile("" : "+v"(csum), "+v"(w2f[0]), "+v"(w2f[1]), "+v"(w2f[2]), "+v"(w2f[3]));
      BAR();
    }
    const unsigned off = WS_HG + (4 * w + (g >> 1)) * 128 + (c & 1) * 64 + (g & 1) * 32;
    v4u d[4];
    for (unsigned spin = 0; spin < SPIN_CAP; ++spin) {
      CBAR();
      d[0] = ldg(rs, off); d[1] = ldg(rs, off + 16); d[2] = ldg(rs, off + 256); d[3] = ldg(rs, off + 272);
      bool ok = true;
#pragma unroll
      for (int i = 0; i < 4; ++i) ok &= (d[i].y == tag) & (d[i].w == tag);
      if (__all(ok)) break;
    }
    STAMP(7);
    {
      const v4u a0 = {d[0].x, d[0].z, d[1].x, d[1].z}, a1 = {d[2].x, d[2].z, d[3].x, d[3].z};
      const f32x4 z0 = mfma(a1, w1f[2], mfma(a0, w1f[0], z4)), z1 = mfma(a1, w1f[3], mfma(a0, w1f[1], z4));
      const float t0 = (g & 1) ? z0[1] : z0[0], t1 = (g & 1) ? z1[1] : z1[0];
      *zdst = (g & 2) ? t1 : t0;
    }
    BAR();
    {
      const f32x4 a = *(LAS f32x4*)(L + L_ZP + lane * 48), bq = *(LAS f32x4*)(L + L_ZP + lane * 48 + 16);
      const f32x2 st = *(LAS f32x2*)(L + L_WN + (lane >> 5) * 8);
      const float zr = ((a[0] + a[1]) + (a[2] + a[3])) + ((bq[0] + bq[1]) + (bq[2] + bq[3]));
      const float z = rbf(rbf(fmaf(-st[0], csum, zr) * st[1]) + bias1);
      const float t = fmaf(z * z, 0.044715f * 0.7978845608028654f * 2.f * LOG2E, 0.7978845608028654f * 2.f * LOG2E);
      const float e = __builtin_amdgcn_exp2f(z * t);
      const float gl = fmaf(-z, __builtin_amdgcn_rcpf(e + 1.0f), z);
      *(LAS u16*)(L + L_GA + w * 160 + (lane >> 5) * 80 + (lane & 31) * 2) = (u16)pk_bf16(gl, gl);
    }
    CBAR();
    const v4u ga = *(LAS v4u*)(L + L_GA + w * 160 + (c & 1) * 80 + g * 16);
    const f32x4 y0 = mfma(ga, w2f[0], z4), y1 = mfma(ga, w2f[1], z4), y2 = mfma(ga, w2f[2], z4), y3 = mfma(ga, w2f[3], z4);
    const float s0 = (g & 1) ? y1[0] : y0[0], s1 = (g & 1) ? y1[1] : y0[1], s2 = (g & 1) ? y3[0] : y2[0], s3 = (g & 1) ? y3[1] : y2[1];
    __builtin_amdgcn_raw_buffer_store_b128(v4u{__float_as_uint((g & 2) ? s2 : s0), tag, __float_as_uint((g & 2) ? s3 : s1), tag}, rs,
                                           WS_PD + ((2 * (4 * w + g) + (c >> 3)) * 56 + f) * 128 + (c & 7) * 16, 0, SC1);
    STAMP(8);
  } else if (j < 31) {
    if (wave == 0 && j < 27) {
      const int r = kvh * 4 + (j - 23);
      const float x0 = bf_lo(p.x[r * 16 + c]), x1 = bf_lo(p.x[512 + r * 16 + c]);
      const unsigned off = WS_PB + (r * 32 + g) * 256 + c * 16;
      v4u d[8];
      for (unsigned spin = 0; spin < SPIN_CAP; ++spin) {
        CBAR();
        bool ok = true;
#pragma unroll
        for (int i = 0; i < 8; ++i) d[i] = ldg(rs, off + i * 1024);
#pragma unroll
        for (int i = 0; i < 8; ++i) ok &= (d[i].y == tag) & (d[i].w == tag);
        if (__all(ok)) break;
      }
      STAMP(6);
#define F_(v) __uint_as_float(v)
      float a0 = ((F_(d[0].x) + F_(d[1].x)) + (F_(d[2].x) + F_(d[3].x))) + ((F_(d[4].x) + F_(d[5].x)) + (F_(d[6].x) + F_(d[7].x)));
      float a1 = ((F_(d[0].z) + F_(d[1].z)) + (F_(d[2].z) + F_(d[3].z))) + ((F_(d[4].z) + F_(d[5].z)) + (F_(d[6].z) + F_(d[7].z)));
      a0 = x32_sum(x16_sum(a0)); a1 = x32_sum(x16_sum(a1));
      const unsigned ar = pk_bf16(a0, a1);
      const float h0 = x0 + bf_lo(ar), h1 = x1 + bf_hi(ar);
      const float n0 = dpp<XOR1>(h0), n1 = dpp<XOR1>(h1);
      const unsigned pk = pk_bf16((c & 1) ? n1 : h0, (c & 1) ? h1 : n0);
      if (g == 0) __builtin_amdgcn_raw_buffer_store_b64(v2u{pk, tag}, rs, WS_HG + r * 128 + ((c & 1) * 8 + (c >> 1)) * 8, 0, SC1);
      if (r == 0 && lane == 0) __builtin_amdgcn_raw_buffer_store_b32(tag, rs, WS_EPOCH, 0, SC1);
      STAMP(7);
    } else if (wave == 1) {
      const int r2 = kvh * 8 + (j - 23), n = lane & 7, fg = lane >> 3;
      const float bias2 = bf_lo(p.b2[r2 * 8 + n]);
      v2u hg0, hg1;
      const unsigned hoff = WS_HG + (r2 >> 1) * 128 + ((r2 & 1) * 4 + (n >> 1)) * 8;
      for (unsigned spin = 0; spin < SPIN_CAP; ++spin) {
        CBAR();
        hg0 = __builtin_amdgcn_raw_buffer_load_b64(rs, hoff, 0, SC1);
        hg1 = __builtin_amdgcn_raw_buffer_load_b64(rs, hoff + 64, 0, SC1);
        if (__all((hg0.y == tag) & (hg1.y == tag))) break;
        __builtin_amdgcn_s_sleep(2);
      }
      STAMP(6);
      const float h0 = (n & 1) ? bf_hi(hg0.x) : bf_lo(hg0.x), h1 = (n & 1) ? bf_hi(hg1.x) : bf_lo(hg1.x);
      const unsigned off = WS_PD + (r2 * 56 + fg) * 128 + n * 16;
      v4u d[7];
      for (unsigned spin = 0; spin < SPIN_CAP; ++spin) {
        CBAR();
        bool ok = true;
#pragma unroll
        for (int i = 0; i < 7; ++i) d[i] = ldg(rs, off + i * 1024);
#pragma unroll
        for (int i = 0; i < 7; ++i) ok &= (d[i].y == tag) & (d[i].w == tag);
        if (__all(ok)) break;
      }
      STAMP(7);
      float a0 = ((F_(d[0].x) + F_(d[1].x)) + (F_(d[2].x) + F_(d[3].x))) + ((F_(d[4].x) + F_(d[5].x)) + F_(d[6].x));
      float a1 = ((F_(d[0].z) + F_(d[1].z)) + (F_(d[2].z) + F_(d[3].z))) + ((F_(d[4].z) + F_(d[5].z)) + F_(d[6].z));
      a0 += dpp<ROR8>(a0); a1 += dpp<ROR8>(a1);
      a0 = x32_sum(x16_sum(a0)); a1 = x32_sum(x16_sum(a1));
      const unsigned ar = pk_bf16(a0, a1);
      const unsigned yr = pk_bf16(bf_lo(ar) + bias2, bf_hi(ar) + bias2);
      const float o0 = h0 + bf_lo(yr), o1 = h1 + bf_hi(yr);
      const float n0 = dpp<XOR1>(o0), n1 = dpp<XOR1>(o1);
      const unsigned pk = pk_bf16((n & 1) ? n1 : o0, (n & 1) ? o1 : n0);
      if (lane < 8) *(unsigned*)(p.out + (n & 1) * 512 + r2 * 8 + (n & 6)) = pk;
      STAMP(8);
    }
  }
  STAMP_FLUSH();
}

}

extern "C" void kernel_launch(void* const* d_in, const int* in_sizes, int n_in,
                              void* d_out, int out_size, void* d_ws, size_t ws_size,
                              hipStream_t stream) {
  Params p;
  p.x = (const u16*)d_in[0]; p.kc = (const u16*)d_in[1]; p.vc = (const u16*)d_in[2];
  p.wq = (const u16*)d_in[3]; p.wo = (const u16*)d_in[4];
  p.w1 = (const u16*)d_in[6]; p.b1 = (const u16*)d_in[7]; p.w2 = (const u16*)d_in[8]; p.b2 = (const u16*)d_in[9];
  p.out = (u16*)d_out; p.ws = (unsigned char*)d_ws;
  (void)hipFuncSetAttribute((const void*)fused_decode, hipFuncAttributeMaxDynamicSharedMemorySize, LDS_BYTES);
  hipLaunchKernelGGL(fused_decode, dim3(256), dim3(NTHR), LDS_BYTES, stream, p);
}
```

```cpp
#define STAMPS 1
#include <hip/hip_runtime.h>
#include <hip/hip_bf16.h>
#include <stdint.h>

typedef unsigned short u16;
typedef unsigned long long u64;
typedef unsigned v4u __attribute__((ext_vector_type(4)));
typedef unsigned v2u __attribute__((ext_vector_type(2)));
typedef float f32x4 __attribute__((ext_vector_type(4)));
typedef float f32x2 __attribute__((ext_vector_type(2)));
typedef short bf16x8 __attribute__((ext_vector_type(8)));
typedef short bf16x4 __attribute__((ext_vector_type(4)));
typedef __bf16 bf16x2 __attribute__((ext_vector_type(2)));
typedef short v4i16 __attribute__((ext_vector_type(4)));
#define LAS __attribute__((address_space(3)))
#define GAS __attribute__((address_space(1)))

namespace {

constexpr int SK = 8192, WIN = 2048, FF = 1792;
constexpr int NTHR = 576;
constexpr int CODE_KB = 12;
constexpr unsigned SPIN_CAP = 1u << 18;
constexpr float LOG2E = 1.4426950408889634f;

constexpr unsigned WS_EPOCH = 0;
constexpr unsigned WS_QG = 4096;
constexpr unsigned WS_PA = WS_QG + 8 * 4 * 2 * 512 * 8;
constexpr unsigned WS_PB = WS_PA + 2 * 32 * 16 * 640;
constexpr unsigned WS_HG = WS_PB + 32 * 32 * 256;
constexpr unsigned WS_PD = WS_HG + 32 * 128;
constexpr unsigned WS_END = WS_PD + 64 * 56 * 128;
constexpr unsigned WS_STAMP = 4u << 20;

constexpr int L_WQ = 0;
constexpr int RED_PITCH = 2080;
constexpr int L_RED = 0;
constexpr int L_W = 17408;
constexpr int L_ACT = L_W + 65536;
constexpr int ACT_PITCH = 256 + 64;
constexpr int L_QS = L_ACT + 2 * ACT_PITCH;
constexpr int QS_PITCH = 256 + 64;
constexpr int L_MX = L_QS + 4 * QS_PITCH;
constexpr int L_LS = L_MX + 128;
constexpr int L_WN = L_LS + 128;
constexpr int L_ZP = L_WN + 128;
constexpr int L_GA = L_ZP + 64 * 48;
constexpr int L_H = L_GA + 8 * 160;
constexpr int H_PITCH = 1024 + 64;
constexpr int L_TAG = L_H + 2 * H_PITCH;
constexpr int L_PF = L_TAG + 64;
constexpr int LDS_BYTES = L_PF + 1024;
static_assert(8 * RED_PITCH <= L_W, "lds map");
static_assert(LDS_BYTES > 81920, "one workgroup per CU");

struct Params {
  const u16* x; const u16* kc; const u16* vc; const u16* wq; const u16* wo;
  const u16* w1; const u16* b1; const u16* w2; const u16* b2;
  u16* out; unsigned char* ws;
};

__device__ __forceinline__ float bf_lo(unsigned d) { return __uint_as_float(d << 16); }
__device__ __forceinline__ float bf_hi(unsigned d) { return __uint_as_float(d & 0xffff0000u); }
__device__ __forceinline__ unsigned pk_bf16(float lo, float hi) {
  return __builtin_bit_cast(unsigned, __builtin_convertvector(f32x2{lo, hi}, bf16x2));
}
__device__ __forceinline__ float rbf(float f) { return bf_lo(pk_bf16(f, f)); }
__device__ __forceinline__ f32x4 mfma(v4u a, v4u b, f32x4 c) {
  return __builtin_amdgcn_mfma_f32_16x16x32_bf16(__builtin_bit_cast(bf16x8, a), __builtin_bit_cast(bf16x8, b), c, 0, 0, 0);
}
__device__ __forceinline__ f32x4 mfma16(v2u a, v2u b, f32x4 c) {
  return __builtin_amdgcn_mfma_f32_16x16x16bf16_1k(__builtin_bit_cast(bf16x4, a), __builtin_bit_cast(bf16x4, b), c, 0, 0, 0);
}
__device__ __forceinline__ float dot2(unsigned a, unsigned b, float acc) {
  return __builtin_amdgcn_fdot2_f32_bf16(__builtin_bit_cast(bf16x2, a), __builtin_bit_cast(bf16x2, b), acc, false);
}
template <int CTRL> __device__ __forceinline__ float dpp(float x) {
  return __builtin_bit_cast(float, __builtin_amdgcn_mov_dpp(__builtin_bit_cast(int, x), CTRL, 0xf, 0xf, true));
}
constexpr int XOR1 = 0xB1, XOR2 = 0x4E, HMIR = 0x141, ROR8 = 0x128, ROR4 = 0x124;
__device__ __forceinline__ float x16_sum(float x) {
  auto s = __builtin_amdgcn_permlane16_swap(__float_as_uint(x), __float_as_uint(x), false, false);
  return __uint_as_float(s[0]) + __uint_as_float(s[1]);
}
__device__ __forceinline__ float x32_sum(float x) {
  auto s = __builtin_amdgcn_permlane32_swap(__float_as_uint(x), __float_as_uint(x), false, false);
  return __uint_as_float(s[0]) + __uint_as_float(s[1]);
}
__device__ __forceinline__ float x16_max(float x) {
  auto s = __builtin_amdgcn_permlane16_swap(__float_as_uint(x), __float_as_uint(x), false, false);
  return fmaxf(__uint_as_float(s[0]), __uint_as_float(s[1]));
}
__device__ __forceinline__ float x32_max(float x) {
  auto s = __builtin_amdgcn_permlane32_swap(__float_as_uint(x), __float_as_uint(x), false, false);
  return fmaxf(__uint_as_float(s[0]), __uint_as_float(s[1]));
}
__device__ __forceinline__ float wave_sum(float x) {
  x += dpp<XOR1>(x); x += dpp<XOR2>(x); x += dpp<HMIR>(x); x += dpp<ROR8>(x);
  return x32_sum(x16_sum(x));
}
__device__ __forceinline__ float wave_sum_par(float x) {
  x += dpp<XOR2>(x); x += dpp<ROR4>(x); x += dpp<ROR8>(x);
  return x32_sum(x16_sum(x));
}
__device__ __forceinline__ float max8(f32x4 a, f32x4 b) {
  return fmaxf(fmaxf(fmaxf(a[0], a[1]), fmaxf(a[2], a[3])), fmaxf(fmaxf(b[0], b[1]), fmaxf(b[2], b[3])));
}
#define BAR() asm volatile("s_waitcnt lgkmcnt(0)\n\ts_barrier" ::: "memory")
#define RBAR() asm volatile("s_barrier" ::: "memory")
#define CBAR() asm volatile("" ::: "memory")
#define LDNT(p) __builtin_nontemporal_load((const v4u*)(p))
#define SC1 16

__device__ __forceinline__ void dma16(const void* sbase, unsigned voff, unsigned ldsaddr) {
  asm volatile("s_mov_b32 m0, %2\n\ts_nop 0\n\tglobal_load_lds_dwordx4 %0, %1 nt" ::"v"(voff), "s"(sbase), "s"(ldsaddr) : "memory");
}
__device__ __forceinline__ v4u tr_frag(LAS unsigned char* p0, LAS unsigned char* p1) {
  v2u a = __builtin_bit_cast(v2u, __builtin_amdgcn_ds_read_tr16_b64_v4i16((LAS v4i16*)p0));
  v2u b = __builtin_bit_cast(v2u, __builtin_amdgcn_ds_read_tr16_b64_v4i16((LAS v4i16*)p1));
  return v4u{a.x, a.y, b.x, b.y};
}
__device__ __forceinline__ v4u ldg(__amdgpu_buffer_rsrc_t rs, unsigned off) { return __builtin_amdgcn_raw_buffer_load_b128(rs, off, 0, SC1); }

template <int N, int STRIDE>
__device__ __forceinline__ void poll(__amdgpu_buffer_rsrc_t rs, unsigned off, unsigned tag, v4u (&d)[N]) {
  v4u e[N];
#pragma unroll
  for (int i = 0; i < N; ++i) d[i] = ldg(rs, off + i * STRIDE);
#pragma unroll 1
  for (unsigned spin = 0; spin < SPIN_CAP; ++spin) {
    CBAR();
#pragma unroll
    for (int i = 0; i < N; ++i) e[i] = ldg(rs, off + i * STRIDE);
    __builtin_amdgcn_sched_barrier(0);
    bool ok = true;
#pragma unroll
    for (int i = 0; i < N; ++i) ok &= (d[i].y == tag) & (d[i].w == tag);
    if (__all(ok)) return;
    CBAR();
#pragma unroll
    for (int i = 0; i < N; ++i) d[i] = ldg(rs, off + i * STRIDE);
    __builtin_amdgcn_sched_barrier(0);
    ok = true;
#pragma unroll
    for (int i = 0; i < N; ++i) ok &= (e[i].y == tag) & (e[i].w == tag);
    if (__all(ok)) {
#pragma unroll
      for (int i = 0; i < N; ++i) d[i] = e[i];
      return;
    }
  }
}

__device__ __forceinline__ v4u ln_row(v4u d) {
  float v[8];
  float s = 0.f;
#pragma unroll
  for (int i = 0; i < 4; ++i) { v[2 * i] = bf_lo(d[i]); v[2 * i + 1] = bf_hi(d[i]); s += v[2 * i] + v[2 * i + 1]; }
  const float mean = wave_sum(s) * (1.0f / 512.0f);
  float q = 0.f;
#pragma unroll
  for (int i = 0; i < 8; ++i) { const float t = v[i] - mean; q = fmaf(t, t, q); }
  q = wave_sum(q);
  const float mb = rbf(mean), vb = rbf(q * (1.0f / 512.0f));
  const float inv = __builtin_amdgcn_rcpf(rbf(__builtin_amdgcn_sqrtf(rbf(vb + 1e-5f))));
  v4u r;
#pragma unroll
  for (int i = 0; i < 4; ++i) {
    const unsigned c = pk_bf16(v[2 * i] - mb, v[2 * i + 1] - mb);
    r[i] = pk_bf16(bf_lo(c) * inv, bf_hi(c) * inv);
  }
  return r;
}

#define STAMP(i) do { __builtin_amdgcn_sched_barrier(0); st_[i] = (unsigned)__builtin_amdgcn_s_memrealtime(); __builtin_amdgcn_sched_barrier(0); } while (0)
#define STAMP_DECL unsigned st_[10] = {0, 0, 0, 0, 0, 0, 0, 0, 0, 0}
#define STAMP_FLUSH() do { if (blockIdx.x == 186 && wave == 1) { const unsigned tgt = 4000u + (st_[0] - st_[0]); while ((unsigned)__builtin_amdgcn_s_memrealtime() - st_[0] < tgt) __builtin_amdgcn_s_sleep(1); } } while (0)

__global__ void __launch_bounds__(NTHR, 1) fused_decode(Params p) {
  extern __shared__ __attribute__((aligned(16))) unsigned char lds_raw[];
  u64 pc;
  asm volatile("s_getpc_b64 %0" : "=s"(pc));
  LAS unsigned char* L = (LAS unsigned char*)lds_raw;
  const unsigned LB = (unsigned)(size_t)L;
  const int tid = threadIdx.x, lane = tid & 63, wave = __builtin_amdgcn_readfirstlane(tid >> 6);
  const int kvh = blockIdx.x & 7, j = blockIdx.x >> 3;
  const int g = lane >> 4, c = lane & 15;
  const __amdgpu_buffer_rsrc_t rs = __builtin_amdgcn_make_buffer_rsrc((void*)p.ws, 0, WS_END, 0x00020000);
  STAMP_DECL;
  STAMP(0);
  unsigned tag;

  if (wave == 0) {
    const int rg = j >> 3, cg = j & 7, b = j >> 4, sp = j & 15;
    unsigned ep; v4u x0, x1;
    asm volatile("global_load_dword %0, %1, %2 sc1" : "=v"(ep) : "v"(WS_EPOCH), "s"(p.ws) : "memory");
    asm volatile("global_load_dwordx4 %0, %1, %2" : "=v"(x0) : "v"(lane * 16), "s"(p.x) : "memory");
    asm volatile("global_load_dwordx4 %0, %1, %2 offset:1024" : "=v"(x1) : "v"(lane * 16), "s"(p.x) : "memory");
    {
      const int r0 = lane >> 3, pos = lane & 7;
      const unsigned v0 = r0 * 8192 + (((pos >> 1) ^ ((r0 >> 1) & 1)) * 32) + (pos & 1) * 16;
      const u16* src = p.wq + (size_t)(rg * 128) * 4096 + kvh * 512 + cg * 64;
#pragma unroll
      for (int i = 0; i < 16; ++i) dma16(src + (size_t)i * 8 * 4096, (i & 1) ? (v0 ^ 64u) : v0, LB + L_WQ + i * 1024);
    }
    if (lane == 0) *(LAS unsigned*)(L + L_TAG + 4) = 0u;
    RBAR();
    asm volatile("s_waitcnt vmcnt(16)" : "+v"(ep), "+v"(x0), "+v"(x1)::"memory");
    tag = __builtin_amdgcn_readfirstlane(ep) + 1u;
    if (lane == 0) *(LAS unsigned*)(L + L_TAG) = tag;
    STAMP(1);
    {
      const v4u a = ln_row(x0), bb = ln_row(x1);
      if (g == rg) {
        *(LAS v4u*)(L + L_ACT + c * 16) = a;
        *(LAS v4u*)(L + L_ACT + ACT_PITCH + c * 16) = bb;
      }
    }
    RBAR();
    v4u af[4];
#pragma unroll
    for (int s = 0; s < 4; ++s) af[s] = *(LAS v4u*)(L + L_ACT + (c & 1) * ACT_PITCH + s * 64 + g * 16);
    f32x4 acc[4] = {};
    {
      const int q4 = (lane >> 2) & 3, pp = lane & 3;
      const int f = ((q4 >> 1) & 1) | ((g & 1) << 1);
      LAS unsigned char* base = L + L_WQ + (8 * g + q4) * 128 + pp * 8;
#pragma unroll
      for (int s = 0; s < 4; ++s) {
        if (s == 0) asm volatile("s_waitcnt vmcnt(12)" ::: "memory");
        if (s == 1) asm volatile("s_waitcnt vmcnt(8)" ::: "memory");
        if (s == 2) asm volatile("s_waitcnt vmcnt(4)" ::: "memory");
        if (s == 3) { asm volatile("s_waitcnt vmcnt(0)" ::: "memory"); STAMP(2); }
#pragma unroll
        for (int t = 0; t < 4; ++t) {
          LAS unsigned char* q = base + s * 4096 + ((t ^ f) * 32);
          acc[t] = mfma(af[s], tr_frag(q, q + 512), acc[t]);
        }
      }
    }
    {
      const float s0 = (g & 1) ? acc[1][0] : acc[0][0], s1 = (g & 1) ? acc[1][1] : acc[0][1];
      const float s2 = (g & 1) ? acc[3][0] : acc[2][0], s3 = (g & 1) ? acc[3][1] : acc[2][1];
      const float q0 = (g & 2) ? s2 : s0, q1 = (g & 2) ? s3 : s1;
      const unsigned off = WS_QG + (((kvh * 4 + rg) * 2) * 512 + cg * 64 + lane) * 8;
      __builtin_amdgcn_raw_buffer_store_b64(v2u{__float_as_uint(q0), tag}, rs, off, 0, SC1);
      __builtin_amdgcn_raw_buffer_store_b64(v2u{__float_as_uint(q1), tag}, rs, off + 4096, 0, SC1);
    }
    STAMP(3);
    RBAR();
    {
      const unsigned off = WS_QG + ((kvh * 4 * 2 + b) * 512 + 2 * lane) * 8;
      v4u d[16];
#pragma unroll
      for (int i = 0; i < 16; ++i) d[i] = ldg(rs, off + (i >> 2) * 8192 + (i & 3) * 1024);
      RBAR();
      for (unsigned spin = 0; spin < SPIN_CAP; ++spin) {
        bool ok = true;
#pragma unroll
        for (int i = 0; i < 16; ++i) ok &= (d[i].y == tag) & (d[i].w == tag);
        if (__all(ok)) break;
        CBAR();
#pragma unroll
        for (int i = 0; i < 16; ++i) d[i] = ldg(rs, off + (i >> 2) * 8192 + (i & 3) * 1024);
      }
#pragma unroll
      for (int hh = 0; hh < 4; ++hh) {
        const float lo = (__uint_as_float(d[hh].x) + __uint_as_float(d[4 + hh].x)) + (__uint_as_float(d[8 + hh].x) + __uint_as_float(d[12 + hh].x));
        const float hi = (__uint_as_float(d[hh].z) + __uint_as_float(d[4 + hh].z)) + (__uint_as_float(d[8 + hh].z) + __uint_as_float(d[12 + hh].z));
        *(LAS unsigned*)(L + L_QS + hh * QS_PITCH + lane * 4) = pk_bf16(lo, hi);
      }
    }
    STAMP(4);
    BAR();
    RBAR();
    {
      const float mw = *(LAS float*)(L + L_MX + g * 32 + (c & 7) * 4), lw = *(LAS float*)(L + L_LS + g * 32 + (c & 7) * 4);
      float M = fmaxf(mw, dpp<XOR1>(mw)); M = fmaxf(M, dpp<XOR2>(M)); M = fmaxf(M, dpp<HMIR>(M));
      float S = lw * __builtin_amdgcn_exp2f(mw - M);
      S += dpp<XOR1>(S); S += dpp<XOR2>(S); S += dpp<HMIR>(S);
      if (c == 0)
        __builtin_amdgcn_raw_buffer_store_b128(v4u{__float_as_uint(M), tag, __float_as_uint(S), tag}, rs,
                                               WS_PA + ((b * 32 + kvh * 4 + g) * 16 + sp) * 640 + 512, 0, SC1);
    }
    if (j < 16) {
      const int h = kvh * 4 + (j >> 2), cq = j & 3;
      const unsigned v0 = g * 1024 + (((c >> 1) ^ g) * 32) + (c & 1) * 16;
      const u16* src = p.wo + (size_t)(h * 128) * 512 + cq * 128;
#pragma unroll
      for (int i = 0; i < 32; ++i) dma16(src + (size_t)i * 4 * 512, (i & 2) ? (v0 ^ 128u) : v0, LB + L_W + i * 1024);
    } else if (j < 23) {
      const int f = kvh * 7 + (j - 16);
      const int r0 = lane >> 2, pos = lane & 3;
      const unsigned v0 = r0 * (FF * 2) + (((pos >> 1) ^ ((r0 >> 3) & 1)) * 32) + (pos & 1) * 16;
      const u16* src = p.w1 + f * 32;
#pragma unroll
      for (int i = 0; i < 32; ++i) dma16(src + (size_t)i * 16 * FF, v0, LB + L_W + i * 1024);
      const u16* src2 = p.w2 + (size_t)(f * 32) * 512;
#pragma unroll
      for (int i = 0; i < 32; ++i)
        dma16(src2 + (size_t)i * 512, (unsigned)(lane * 16) ^ (unsigned)(((i & 3) | (((i >> 3) & 1) << 2)) * 32), LB + L_W + 32768 + i * 1024);
    }
  } else {
    const int w = wave - 1, b = j >> 4, sp = j & 15;
    if (w == 7 && j < CODE_KB) {
      asm volatile("s_mov_b32 m0, %2\n\ts_nop 0\n\tglobal_load_lds_dwordx4 %0, %1" ::"v"(j * 1024 + lane * 16), "s"(pc & ~255ull), "s"(LB + L_PF) : "memory");
    }
    const size_t base = ((size_t)(b * 8 + kvh) * SK + (SK - WIN) + sp * 128 + w * 16) * 128;
    const u16* kp = p.kc + base + c * 128 + g * 8;
    const u16* vp = p.vc + base + (4 * g) * 128 + c * 8;
    v4u kf[4], vf[4];
    RBAR();
#pragma unroll
    for (int s = 0; s < 4; ++s) kf[s] = LDNT(kp + s * 32);
    RBAR();
    vf[0] = LDNT(vp); vf[1] = LDNT(vp + 128); vf[2] = LDNT(vp + 256);
    asm volatile("" : "+v"(kf[0]), "+v"(kf[1]), "+v"(kf[2]), "+v"(kf[3])::"memory");
    STAMP(1);
    RBAR();
    RBAR();
    vf[3] = LDNT(vp + 384);
    const unsigned mk0 = (c & 2) ? 0u : ((c & 1) ? 0xffff0000u : 0x0000ffffu), mk1 = (c & 2) ? ((c & 1) ? 0xffff0000u : 0x0000ffffu) : 0u;
    RBAR();
    STAMP(2);
    tag = *(LAS unsigned*)(L + L_TAG);
    f32x4 sc = {};
    v4u qf[4];
#pragma unroll
    for (int s = 0; s < 4; ++s) qf[s] = *(LAS v4u*)(L + L_QS + (c >> 2) * QS_PITCH + s * 64 + g * 16);
    __builtin_amdgcn_sched_barrier(0);
#pragma unroll
    for (int s = 0; s < 4; ++s) sc = mfma(kf[s], qf[s], sc);
    float sv[4];
    {
      const unsigned r01 = pk_bf16(sc[0], sc[1]), r23 = pk_bf16(sc[2], sc[3]);
      const float k = 0.08838834764831845f;
      const unsigned t01 = pk_bf16(bf_lo(r01) * k, bf_hi(r01) * k), t23 = pk_bf16(bf_lo(r23) * k, bf_hi(r23) * k);
      sv[0] = bf_lo(t01); sv[1] = bf_hi(t01); sv[2] = bf_lo(t23); sv[3] = bf_hi(t23);
    }
    const float mw = x32_max(x16_max(fmaxf(fmaxf(sv[0], sv[1]), fmaxf(sv[2], sv[3]))));
    v2u pa[4];
    float lw = 0.f;
#pragma unroll
    for (int t = 0; t < 4; ++t) {
      const float e = __builtin_amdgcn_exp2f((sv[t] - mw) * LOG2E);
      lw += e;
      const unsigned pk = pk_bf16(e, e);
      pa[t] = v2u{pk & mk0, pk & mk1};
    }
    lw = x32_sum(x16_sum(lw));
    if ((lane & 51) == 0) {
      *(LAS float*)(L + L_MX + (c >> 2) * 32 + w * 4) = mw * LOG2E;
      *(LAS float*)(L + L_LS + (c >> 2) * 32 + w * 4) = lw;
    }
    STAMP(3);
    BAR();
    const float fac = __builtin_amdgcn_exp2f(*(LAS float*)(L + L_MX + g * 32 + w * 4) -
                                             max8(*(LAS f32x4*)(L + L_MX + g * 32), *(LAS f32x4*)(L + L_MX + g * 32 + 16)));
    f32x4 olo = {}, ohi = {};
#pragma unroll
    for (int t = 0; t < 4; ++t) { olo = mfma16(pa[t], v2u{vf[t].x, vf[t].y}, olo); ohi = mfma16(pa[t], v2u{vf[t].z, vf[t].w}, ohi); }
    *(LAS f32x4*)(L + L_RED + w * RED_PITCH + g * 512 + c * 16) = olo * fac;
    *(LAS f32x4*)(L + L_RED + w * RED_PITCH + g * 512 + 256 + c * 16) = ohi * fac;
    STAMP(4);
    {
      CBAR();
      unsigned cnt = 0;
      if (lane == 0) cnt = __hip_atomic_fetch_add((LAS unsigned*)(L + L_TAG + 4), 1u, __ATOMIC_RELAXED, __HIP_MEMORY_SCOPE_WORKGROUP) + 1u;
      cnt = __builtin_amdgcn_readfirstlane(cnt);
      while (cnt < 8u) cnt = __builtin_amdgcn_readfirstlane(*(volatile LAS unsigned*)(L + L_TAG + 4));
      CBAR();
    }
    {
      const int hh = w >> 1, pr = (w & 1) * 32 + (lane >> 1), half = lane & 1;
      LAS unsigned char* src = L + L_RED + (4 * half) * RED_PITCH + hh * 512 + ((pr >> 1) & 1) * 256 + (pr >> 2) * 16 + (pr & 1) * 8;
      f32x2 a = (*(LAS f32x2*)(src) + *(LAS f32x2*)(src + RED_PITCH)) + (*(LAS f32x2*)(src + 2 * RED_PITCH) + *(LAS f32x2*)(src + 3 * RED_PITCH));
      a[0] += dpp<XOR1>(a[0]); a[1] += dpp<XOR1>(a[1]);
      if (half == 0)
        __builtin_amdgcn_raw_buffer_store_b64(v2u{pk_bf16(a[0], a[1]), tag}, rs, WS_PA + ((b * 32 + kvh * 4 + hh) * 16 + sp) * 640 + pr * 8, 0, SC1);
    }
    STAMP(5);
  }

  if (j < 16) {
    const int h = kvh * 4 + (j >> 2), cq = j & 3;
    if (wave == 0) {
      const unsigned off = WS_PA + (((g & 1) * 32 + h) * 16 + c) * 640 + 512;
      v4u d;
      for (unsigned spin = 0; spin < SPIN_CAP; ++spin) {
        CBAR();
        d = ldg(rs, off);
        if (__all((d.y == tag) & (d.w == tag))) break;
      }
      const float m = __uint_as_float(d.x);
      float M = fmaxf(m, dpp<XOR1>(m)); M = fmaxf(M, dpp<XOR2>(M)); M = fmaxf(M, dpp<HMIR>(M)); M = fmaxf(M, dpp<ROR8>(M));
      const float wv = __builtin_amdgcn_exp2f(m - M);
      float S = wv * __uint_as_float(d.z);
      S += dpp<XOR1>(S); S += dpp<XOR2>(S); S += dpp<HMIR>(S); S += dpp<ROR8>(S);
      if (g < 2) *(LAS float*)(L + L_WN + lane * 4) = wv / S;
      asm volatile("s_waitcnt vmcnt(0)" ::: "memory");
      STAMP(6);
      BAR();
      STAMP_FLUSH();
      return;
    }
    const int w = wave - 1;
    const int b = w >> 2, dq = w & 3, s8 = lane & 7, xx = lane >> 3;
    const unsigned off = WS_PA + ((b * 32 + h) * 16 + s8) * 640 + dq * 128 + xx * 16;
    v4u d0 = ldg(rs, off), d1 = ldg(rs, off + 8 * 640);
    RBAR();
    v4u wf[4];
    {
      const int q4 = (lane >> 2) & 3, pp = lane & 3;
      const int f = q4 | ((g & 1) << 2);
#pragma unroll
      for (int s = 0; s < 4; ++s) {
        LAS unsigned char* base = L + L_W + (32 * s + 8 * g + q4) * 256 + ((w ^ f) * 32) + pp * 8;
        wf[s] = tr_frag(base, base + 1024);
      }
    }
    float w0 = *(LAS float*)(L + L_WN + b * 64 + s8 * 4), w1 = *(LAS float*)(L + L_WN + b * 64 + 32 + s8 * 4);
    asm volatile("" : "+v"(wf[0]), "+v"(wf[1]), "+v"(wf[2]), "+v"(wf[3]), "+v"(w0), "+v"(w1));
    if (!__all((d0.y == tag) & (d0.w == tag) & (d1.y == tag) & (d1.w == tag))) {
      v4u d[2];
      poll<2, 8 * 640>(rs, off, tag, d);
      d0 = d[0]; d1 = d[1];
    }
    STAMP(6);
    {
      const float v0 = w0 * bf_lo(d0.x) + w1 * bf_lo(d1.x), v1 = w0 * bf_hi(d0.x) + w1 * bf_hi(d1.x);
      const float v2 = w0 * bf_lo(d0.z) + w1 * bf_lo(d1.z), v3 = w0 * bf_hi(d0.z) + w1 * bf_hi(d1.z);
      const float a0 = v0 + dpp<HMIR>(v0), a1 = v1 + dpp<HMIR>(v1), a2 = v2 + dpp<HMIR>(v2), a3 = v3 + dpp<HMIR>(v3);
      float u0 = (s8 & 4) ? a2 : a0, u1 = (s8 & 4) ? a3 : a1;
      u0 += dpp<XOR2>(u0); u1 += dpp<XOR2>(u1);
      u0 += dpp<XOR1>(u0); u1 += dpp<XOR1>(u1);
      if ((s8 & 3) == 0) *(LAS unsigned*)(L + L_ACT + b * ACT_PITCH + (dq * 32 + xx * 4 + (s8 >> 2) * 2) * 2) = pk_bf16(u0, u1);
    }
    BAR();
    f32x4 acc = {};
    v4u of[4];
#pragma unroll
    for (int s = 0; s < 4; ++s) of[s] = *(LAS v4u*)(L + L_ACT + (c & 1) * ACT_PITCH + s * 64 + g * 16);
    __builtin_amdgcn_sched_barrier(0);
#pragma unroll
    for (int s = 0; s < 4; ++s) acc = mfma(of[s], wf[s], acc);
    if (g == 0)
      __builtin_amdgcn_raw_buffer_store_b128(v4u{__float_as_uint(acc[0]), tag, __float_as_uint(acc[1]), tag}, rs,
                                             WS_PB + ((cq * 8 + w) * 32 + h) * 256 + c * 16, 0, SC1);
    STAMP(7);
  } else if (j < 23) {
    const int f = kvh * 7 + (j - 16);
    if (wave == 0) {
      asm volatile("s_waitcnt vmcnt(0)" ::: "memory");
      STAMP(6);
      RBAR();
      RBAR();
      const unsigned off = WS_HG + lane * 64;
      v4u d[4];
      poll<4, 16>(rs, off, tag, d);
      STAMP(7);
      {
        LAS unsigned char* dst = L + L_H + (lane & 1) * H_PITCH + (lane >> 1) * 32;
        *(LAS v4u*)dst = v4u{d[0].x, d[0].z, d[1].x, d[1].z};
        *(LAS v4u*)(dst + 16) = v4u{d[2].x, d[2].z, d[3].x, d[3].z};
      }
      BAR();
      float s0 = 0.f, s1 = 0.f, q0 = 0.f, q1 = 0.f;
#pragma unroll
      for (int i = 0; i < 4; ++i) {
        s0 = dot2(d[i].x, 0x3f803f80u, s0); s1 = dot2(d[i].z, 0x3f803f80u, s1);
        q0 = dot2(d[i].x, d[i].x, q0); q1 = dot2(d[i].z, d[i].z, q1);
      }
      const float mean = wave_sum_par(s0 + s1) * (1.0f / 512.0f);
      const float ex2 = wave_sum_par(q0 + q1) * (1.0f / 512.0f);
      const float var = fmaxf(fmaf(-mean, mean, ex2), 0.f);
      const float inv = __builtin_amdgcn_rcpf(rbf(__builtin_amdgcn_sqrtf(rbf(rbf(var) + 1e-5f))));
      if (lane < 2) *(LAS f32x2*)(L + L_WN + lane * 8) = f32x2{rbf(mean), inv};
      BAR();
      STAMP(8);
      STAMP_FLUSH();
      return;
    }
    const int w = wave - 1;
    const float bias1 = bf_lo(p.b1[f * 32 + (lane & 31)]);
    RBAR();
    v4u w1f[4], w2f[4];
    {
      const int q4 = (lane >> 2) & 3, pp = lane & 3;
#pragma unroll
      for (int s = 0; s < 4; ++s) {
        LAS unsigned char* base = L + L_W + (64 * w + 32 * (s >> 1) + 8 * g + q4) * 64 + (((s & 1) ^ (g & 1)) * 32) + pp * 8;
        w1f[s] = tr_frag(base, base + 256);
      }
      const int fz = q4 | ((g & 1) << 2);
#pragma unroll
      for (int t = 0; t < 4; ++t) {
        LAS unsigned char* base = L + L_W + 32768 + (8 * g + q4) * 1024 + (((4 * w + t) ^ fz) * 32) + pp * 8;
        w2f[t] = tr_frag(base, base + 4096);
      }
    }
    const f32x4 z4 = {};
    LAS float* zdst = (LAS float*)(L + L_ZP + ((g & 1) * 32 + (g >> 1) * 16 + c) * 48 + w * 4);
    float csum;
    {
      const v4u ones = {0x3f803f80u, 0x3f803f80u, 0x3f803f80u, 0x3f803f80u};
      const f32x4 c0 = mfma(ones, w1f[2], mfma(ones, w1f[0], z4)), c1 = mfma(ones, w1f[3], mfma(ones, w1f[1], z4));
      *zdst = (g & 2) ? c1[0] : c0[0];
      BAR();
      const f32x4 a = *(LAS f32x4*)(L + L_ZP + lane * 48), bq = *(LAS f32x4*)(L + L_ZP + lane * 48 + 16);
      csum = ((a[0] + a[1]) + (a[2] + a[3])) + ((bq[0] + bq[1]) + (bq[2] + bq[3]));
      asm volatile("" : "+v"(csum), "+v"(w2f[0]), "+v"(w2f[1]), "+v"(w2f[2]), "+v"(w2f[3]));
    }
    BAR();
    STAMP(7);
    {
      const v4u a0 = *(LAS v4u*)(L + L_H + (c & 1) * H_PITCH + w * 128 + g * 16), a1 = *(LAS v4u*)(L + L_H + (c & 1) * H_PITCH + w * 128 + 64 + g * 16);
      const f32x4 z0 = mfma(a1, w1f[2], mfma(a0, w1f[0], z4)), z1 = mfma(a1, w1f[3], mfma(a0, w1f[1], z4));
      const float t0 = (g & 1) ? z0[1] : z0[0], t1 = (g & 1) ? z1[1] : z1[0];
      *zdst = (g & 2) ? t1 : t0;
    }
    BAR();
    {
      const f32x4 a = *(LAS f32x4*)(L + L_ZP + lane * 48), bq = *(LAS f32x4*)(L + L_ZP + lane * 48 + 16);
      const f32x2 st = *(LAS f32x2*)(L + L_WN + (lane >> 5) * 8);
      const float zr = ((a[0] + a[1]) + (a[2] + a[3])) + ((bq[0] + bq[1]) + (bq[2] + bq[3]));
      const float z = rbf(rbf(fmaf(-st[0], csum, zr) * st[1]) + bias1);
      const float t = fmaf(z * z, 0.044715f * 0.7978845608028654f * 2.f * LOG2E, 0.7978845608028654f * 2.f * LOG2E);
      const float e = __builtin_amdgcn_exp2f(z * t);
      const float gl = fmaf(-z, __builtin_amdgcn_rcpf(e + 1.0f), z);
      *(LAS u16*)(L + L_GA + w * 160 + (lane >> 5) * 80 + (lane & 31) * 2) = (u16)pk_bf16(gl, gl);
    }
    CBAR();
    const v4u ga = *(LAS v4u*)(L + L_GA + w * 160 + (c & 1) * 80 + g * 16);
    const f32x4 y0 = mfma(ga, w2f[0], z4), y1 = mfma(ga, w2f[1], z4), y2 = mfma(ga, w2f[2], z4), y3 = mfma(ga, w2f[3], z4);
    const float s0 = (g & 1) ? y1[0] : y0[0], s1 = (g & 1) ? y1[1] : y0[1], s2 = (g & 1) ? y3[0] : y2[0], s3 = (g & 1) ? y3[1] : y2[1];
    __builtin_amdgcn_raw_buffer_store_b128(v4u{__float_as_uint((g & 2) ? s2 : s0), tag, __float_as_uint((g & 2) ? s3 : s1), tag}, rs,
                                           WS_PD + ((2 * (4 * w + g) + (c >> 3)) * 56 + f) * 128 + (c & 7) * 16, 0, SC1);
    STAMP(8);
  } else if (j < 31) {
    if (wave == 0 && j < 27) {
      const int r = kvh * 4 + (j - 23);
      const float x0 = bf_lo(p.x[r * 16 + c]), x1 = bf_lo(p.x[512 + r * 16 + c]);
      const unsigned off = WS_PB + (r * 32 + g) * 256 + c * 16;
      v4u d[8];
      poll<8, 1024>(rs, off, tag, d);
      STAMP(6);
#define F_(v) __uint_as_float(v)
      float a0 = ((F_(d[0].x) + F_(d[1].x)) + (F_(d[2].x) + F_(d[3].x))) + ((F_(d[4].x) + F_(d[5].x)) + (F_(d[6].x) + F_(d[7].x)));
      float a1 = ((F_(d[0].z) + F_(d[1].z)) + (F_(d[2].z) + F_(d[3].z))) + ((F_(d[4].z) + F_(d[5].z)) + (F_(d[6].z) + F_(d[7].z)));
      a0 = x32_sum(x16_sum(a0)); a1 = x32_sum(x16_sum(a1));
      const unsigned ar = pk_bf16(a0, a1);
      const float h0 = x0 + bf_lo(ar), h1 = x1 + bf_hi(ar);
      const float n0 = dpp<XOR1>(h0), n1 = dpp<XOR1>(h1);
      const unsigned pk = pk_bf16((c & 1) ? n1 : h0, (c & 1) ? h1 : n0);
      if (g == 0) __builtin_amdgcn_raw_buffer_store_b64(v2u{pk, tag}, rs, WS_HG + r * 128 + ((c & 1) * 8 + (c >> 1)) * 8, 0, SC1);
      if (r == 0 && lane == 0) __builtin_amdgcn_raw_buffer_store_b32(tag, rs, WS_EPOCH, 0, SC1);
      STAMP(7);
    } else if (wave == 1) {
      const int r2 = kvh * 8 + (j - 23), n = lane & 7, fg = lane >> 3;
      const float bias2 = bf_lo(p.b2[r2 * 8 + n]);
      v2u hg0, hg1;
      const unsigned hoff = WS_HG + (r2 >> 1) * 128 + ((r2 & 1) * 4 + (n >> 1)) * 8;
      for (unsigned spin = 0; spin < SPIN_CAP; ++spin) {
        CBAR();
        hg0 = __builtin_amdgcn_raw_buffer_load_b64(rs, hoff, 0, SC1);
        hg1 = __builtin_amdgcn_raw_buffer_load_b64(rs, hoff + 64, 0, SC1);
        if (__all((hg0.y == tag) & (hg1.y == tag))) break;
        __builtin_amdgcn_s_sleep(2);
      }
      STAMP(6);
      const float h0 = (n & 1) ? bf_hi(hg0.x) : bf_lo(hg0.x), h1 = (n & 1) ? bf_hi(hg1.x) : bf_lo(hg1.x);
      const unsigned off = WS_PD + (r2 * 56 + fg) * 128 + n * 16;
      v4u d[7];
      poll<7, 1024>(rs, off, tag, d);
      STAMP(7);
      float a0 = ((F_(d[0].x) + F_(d[1].x)) + (F_(d[2].x) + F_(d[3].x))) + ((F_(d[4].x) + F_(d[5].x)) + F_(d[6].x));
      float a1 = ((F_(d[0].z) + F_(d[1].z)) + (F_(d[2].z) + F_(d[3].z))) + ((F_(d[4].z) + F_(d[5].z)) + F_(d[6].z));
      a0 += dpp<ROR8>(a0); a1 += dpp<ROR8>(a1);
      a0 = x32_sum(x16_sum(a0)); a1 = x32_sum(x16_sum(a1));
      const unsigned ar = pk_bf16(a0, a1);
      const unsigned yr = pk_bf16(bf_lo(ar) + bias2, bf_hi(ar) + bias2);
      const float o0 = h0 + bf_lo(yr), o1 = h1 + bf_hi(yr);
      const float n0 = dpp<XOR1>(o0), n1 = dpp<XOR1>(o1);
      const unsigned pk = pk_bf16((n & 1) ? n1 : o0, (n & 1) ? o1 : n0);
      if (lane < 8) *(unsigned*)(p.out + (n & 1) * 512 + r2 * 8 + (n & 6)) = pk;
      STAMP(8);
    }
  }
  STAMP_FLUSH();
}

}

extern "C" void kernel_launch(void* const* d_in, const int* in_sizes, int n_in,
                              void* d_out, int out_size, void* d_ws, size_t ws_size,
                              hipStream_t stream) {
  Params p;
  p.x = (const u16*)d_in[0]; p.kc = (const u16*)d_in[1]; p.vc = (const u16*)d_in[2];
  p.wq = (const u16*)d_in[3]; p.wo = (const u16*)d_in[4];
  p.w1 = (const u16*)d_in[6]; p.b1 = (const u16*)d_in[7]; p.w2 = (const u16*)d_in[8]; p.b2 = (const u16*)d_in[9];
  p.out = (u16*)d_out; p.ws = (unsigned char*)d_ws;
  (void)hipFuncSetAttribute((const void*)fused_decode, hipFuncAttributeMaxDynamicSharedMemorySize, LDS_BYTES);
  hipLaunchKernelGGL(fused_decode, dim3(256), dim3(NTHR), LDS_BYTES, stream, p);
}
```

```cpp
#include <hip/hip_runtime.h>
#include <hip/hip_bf16.h>
#include <stdint.h>

typedef unsigned short u16;
typedef unsigned long long u64;
typedef unsigned v4u __attribute__((ext_vector_type(4)));
typedef unsigned v2u __attribute__((ext_vector_type(2)));
typedef float f32x4 __attribute__((ext_vector_type(4)));
typedef float f32x2 __attribute__((ext_vector_type(2)));
typedef short bf16x8 __attribute__((ext_vector_type(8)));
typedef short bf16x4 __attribute__((ext_vector_type(4)));
typedef __bf16 bf16x2 __attribute__((ext_vector_type(2)));
typedef short v4i16 __attribute__((ext_vector_type(4)));
#define LAS __attribute__((address_space(3)))
#define GAS __attribute__((address_space(1)))

namespace {

constexpr int SK = 8192, WIN = 2048, FF = 1792;
constexpr int NTHR = 576;
constexpr int CODE_KB = 12;
constexpr unsigned SPIN_CAP = 1u << 18;
constexpr float LOG2E = 1.4426950408889634f;

constexpr unsigned WS_EPOCH = 0;
constexpr unsigned WS_QG = 4096;
constexpr unsigned WS_PA = WS_QG + 8 * 4 * 2 * 512 * 8;
constexpr unsigned WS_PB = WS_PA + 2 * 32 * 16 * 640;
constexpr unsigned WS_HG = WS_PB + 32 * 32 * 256;
constexpr unsigned WS_PD = WS_HG + 32 * 128;
constexpr unsigned WS_END = WS_PD + 64 * 56 * 128;
constexpr unsigned WS_STAMP = 4u << 20;

constexpr int L_WQ = 0;
constexpr int RED_PITCH = 2080;
constexpr int L_RED = 0;
constexpr int L_W = 17408;
constexpr int L_ACT = L_W + 65536;
constexpr int ACT_PITCH = 256 + 64;
constexpr int L_QS = L_ACT + 2 * ACT_PITCH;
constexpr int QS_PITCH = 256 + 64;
constexpr int L_MX = L_QS + 4 * QS_PITCH;
constexpr int L_LS = L_MX + 128;
constexpr int L_WN = L_LS + 128;
constexpr int L_ZP = L_WN + 128;
constexpr int L_GA = L_ZP + 64 * 48;
constexpr int L_H = L_GA + 8 * 160;
constexpr int H_PITCH = 1024 + 64;
constexpr int L_TAG = L_H + 2 * H_PITCH;
constexpr int L_PF = L_TAG + 64;
constexpr int LDS_BYTES = L_PF + 1024;
static_assert(8 * RED_PITCH <= L_W, "lds map");
static_assert(LDS_BYTES > 81920, "one workgroup per CU");

struct Params {
  const u16* x; const u16* kc; const u16* vc; const u16* wq; const u16* wo;
  const u16* w1; const u16* b1; const u16* w2; const u16* b2;
  u16* out; unsigned char* ws;
};

__device__ __forceinline__ float bf_lo(unsigned d) { return __uint_as_float(d << 16); }
__device__ __forceinline__ float bf_hi(unsigned d) { return __uint_as_float(d & 0xffff0000u); }
__device__ __forceinline__ unsigned pk_bf16(float lo, float hi) {
  return __builtin_bit_cast(unsigned, __builtin_convertvector(f32x2{lo, hi}, bf16x2));
}
__device__ __forceinline__ float rbf(float f) { return bf_lo(pk_bf16(f, f)); }
__device__ __forceinline__ f32x4 mfma(v4u a, v4u b, f32x4 c) {
  return __builtin_amdgcn_mfma_f32_16x16x32_bf16(__builtin_bit_cast(bf16x8, a), __builtin_bit_cast(bf16x8, b), c, 0, 0, 0);
}
__device__ __forceinline__ f32x4 mfma16(v2u a, v2u b, f32x4 c) {
  return __builtin_amdgcn_mfma_f32_16x16x16bf16_1k(__builtin_bit_cast(bf16x4, a), __builtin_bit_cast(bf16x4, b), c, 0, 0, 0);
}
__device__ __forceinline__ float dot2(unsigned a, unsigned b, float acc) {
  return __builtin_amdgcn_fdot2_f32_bf16(__builtin_bit_cast(bf16x2, a), __builtin_bit_cast(bf16x2, b), acc, false);
}
template <int CTRL> __device__ __forceinline__ float dpp(float x) {
  return __builtin_bit_cast(float, __builtin_amdgcn_mov_dpp(__builtin_bit_cast(int, x), CTRL, 0xf, 0xf, true));
}
constexpr int XOR1 = 0xB1, XOR2 = 0x4E, HMIR = 0x141, ROR8 = 0x128, ROR4 = 0x124;
__device__ __forceinline__ float x16_sum(float x) {
  auto s = __builtin_amdgcn_permlane16_swap(__float_as_uint(x), __float_as_uint(x), false, false);
  return __uint_as_float(s[0]) + __uint_as_float(s[1]);
}
__device__ __forceinline__ float x32_sum(float x) {
  auto s = __builtin_amdgcn_permlane32_swap(__float_as_uint(x), __float_as_uint(x), false, false);
  return __uint_as_float(s[0]) + __uint_as_float(s[1]);
}
__device__ __forceinline__ float x16_max(float x) {
  auto s = __builtin_amdgcn_permlane16_swap(__float_as_uint(x), __float_as_uint(x), false, false);
  return fmaxf(__uint_as_float(s[0]), __uint_as_float(s[1]));
}
__device__ __forceinline__ float x32_max(float x) {
  auto s = __builtin_amdgcn_permlane32_swap(__float_as_uint(x), __float_as_uint(x), false, false);
  return fmaxf(__uint_as_float(s[0]), __uint_as_float(s[1]));
}
__device__ __forceinline__ float wave_sum(float x) {
  x += dpp<XOR1>(x); x += dpp<XOR2>(x); x += dpp<HMIR>(x); x += dpp<ROR8>(x);
  return x32_sum(x16_sum(x));
}
__device__ __forceinline__ float wave_sum_par(float x) {
  x += dpp<XOR2>(x); x += dpp<ROR4>(x); x += dpp<ROR8>(x);
  return x32_sum(x16_sum(x));
}
__device__ __forceinline__ float max8(f32x4 a, f32x4 b) {
  return fmaxf(fmaxf(fmaxf(a[0], a[1]), fmaxf(a[2], a[3])), fmaxf(fmaxf(b[0], b[1]), fmaxf(b[2], b[3])));
}
#define BAR() asm volatile("s_waitcnt lgkmcnt(0)\n\ts_barrier" ::: "memory")
#define RBAR() asm volatile("s_barrier" ::: "memory")
#define CBAR() asm volatile("" ::: "memory")
#define LDNT(p) __builtin_nontemporal_load((const v4u*)(p))
#define SC1 16

__device__ __forceinline__ void dma16(const void* sbase, unsigned voff, unsigned ldsaddr) {
  asm volatile("s_mov_b32 m0, %2\n\ts_nop 0\n\tglobal_load_lds_dwordx4 %0, %1 nt" ::"v"(voff), "s"(sbase), "s"(ldsaddr) : "memory");
}
__device__ __forceinline__ v4u tr_frag(LAS unsigned char* p0, LAS unsigned char* p1) {
  v2u a = __builtin_bit_cast(v2u, __builtin_amdgcn_ds_read_tr16_b64_v4i16((LAS v4i16*)p0));
  v2u b = __builtin_bit_cast(v2u, __builtin_amdgcn_ds_read_tr16_b64_v4i16((LAS v4i16*)p1));
  return v4u{a.x, a.y, b.x, b.y};
}
__device__ __forceinline__ v4u ldg(__amdgpu_buffer_rsrc_t rs, unsigned off) { return __builtin_amdgcn_raw_buffer_load_b128(rs, off, 0, SC1); }

template <int N, int STRIDE>
__device__ __forceinline__ void poll(__amdgpu_buffer_rsrc_t rs, unsigned off, unsigned tag, v4u (&d)[N]) {
  v4u e[N];
#pragma unroll
  for (int i = 0; i < N; ++i) d[i] = ldg(rs, off + i * STRIDE);
#pragma unroll 1
  for (unsigned spin = 0; spin < SPIN_CAP; ++spin) {
    CBAR();
#pragma unroll
    for (int i = 0; i < N; ++i) e[i] = ldg(rs, off + i * STRIDE);
    __builtin_amdgcn_sched_barrier(0);
    bool ok = true;
#pragma unroll
    for (int i = 0; i < N; ++i) ok &= (d[i].y == tag) & (d[i].w == tag);
    if (__all(ok)) return;
    CBAR();
#pragma unroll
    for (int i = 0; i < N; ++i) d[i] = ldg(rs, off + i * STRIDE);
    __builtin_amdgcn_sched_barrier(0);
    ok = true;
#pragma unroll
    for (int i = 0; i < N; ++i) ok &= (e[i].y == tag) & (e[i].w == tag);
    if (__all(ok)) {
#pragma unroll
      for (int i = 0; i < N; ++i) d[i] = e[i];
      return;
    }
  }
}

__device__ __forceinline__ v4u ln_row(v4u d) {
  float v[8];
  float s = 0.f;
#pragma unroll
  for (int i = 0; i < 4; ++i) { v[2 * i] = bf_lo(d[i]); v[2 * i + 1] = bf_hi(d[i]); s += v[2 * i] + v[2 * i + 1]; }
  const float mean = wave_sum(s) * (1.0f / 512.0f);
  float q = 0.f;
#pragma unroll
  for (int i = 0; i < 8; ++i) { const float t = v[i] - mean; q = fmaf(t, t, q); }
  q = wave_sum(q);
  const float mb = rbf(mean), vb = rbf(q * (1.0f / 512.0f));
  const float inv = __builtin_amdgcn_rcpf(rbf(__builtin_amdgcn_sqrtf(rbf(vb + 1e-5f))));
  v4u r;
#pragma unroll
  for (int i = 0; i < 4; ++i) {
    const unsigned c = pk_bf16(v[2 * i] - mb, v[2 * i + 1] - mb);
    r[i] = pk_bf16(bf_lo(c) * inv, bf_hi(c) * inv);
  }
  return r;
}

#define STAMP(i) do {} while (0)
#define STAMP_DECL
#define STAMP_FLUSH() do {} while (0)

__global__ void __launch_bounds__(NTHR, 1) fused_decode(Params p) {
  extern __shared__ __attribute__((aligned(16))) unsigned char lds_raw[];
  u64 pc;
  asm volatile("s_getpc_b64 %0" : "=s"(pc));
  LAS unsigned char* L = (LAS unsigned char*)lds_raw;
  const unsigned LB = (unsigned)(size_t)L;
  const int tid = threadIdx.x, lane = tid & 63, wave = __builtin_amdgcn_readfirstlane(tid >> 6);
  const int kvh = blockIdx.x & 7, j = blockIdx.x >> 3;
  const int g = lane >> 4, c = lane & 15;
  const __amdgpu_buffer_rsrc_t rs = __builtin_amdgcn_make_buffer_rsrc((void*)p.ws, 0, WS_END, 0x00020000);
  STAMP_DECL;
  STAMP(0);
  unsigned tag;

  if (wave == 0) {
    const int rg = j >> 3, cg = j & 7, b = j >> 4, sp = j & 15;
    unsigned ep; v4u x0, x1;
    asm volatile("global_load_dword %0, %1, %2 sc1" : "=v"(ep) : "v"(WS_EPOCH), "s"(p.ws) : "memory");
    asm volatile("global_load_dwordx4 %0, %1, %2" : "=v"(x0) : "v"(lane * 16), "s"(p.x) : "memory");
    asm volatile("global_load_dwordx4 %0, %1, %2 offset:1024" : "=v"(x1) : "v"(lane * 16), "s"(p.x) : "memory");
    {
      const int r0 = lane >> 3, pos = lane & 7;
      const unsigned v0 = r0 * 8192 + (((pos >> 1) ^ ((r0 >> 1) & 1)) * 32) + (pos & 1) * 16;
      const u16* src = p.wq + (size_t)(rg * 128) * 4096 + kvh * 512 + cg * 64;
#pragma unroll
      for (int i = 0; i < 16; ++i) dma16(src + (size_t)i * 8 * 4096, (i & 1) ? (v0 ^ 64u) : v0, LB + L_WQ + i * 1024);
    }
    if (lane == 0) *(LAS unsigned*)(L + L_TAG + 4) = 0u;
    RBAR();
    asm volatile("s_waitcnt vmcnt(16)" : "+v"(ep), "+v"(x0), "+v"(x1)::"memory");
    tag = __builtin_amdgcn_readfirstlane(ep) + 1u;
    if (lane == 0) *(LAS unsigned*)(L + L_TAG) = tag;
    STAMP(1);
    {
      const v4u a = ln_row(x0), bb = ln_row(x1);
      if (g == rg) {
        *(LAS v4u*)(L + L_ACT + c * 16) = a;
        *(LAS v4u*)(L + L_ACT + ACT_PITCH + c * 16) = bb;
      }
    }
    RBAR();
    v4u af[4];
#pragma unroll
    for (int s = 0; s < 4; ++s) af[s] = *(LAS v4u*)(L + L_ACT + (c & 1) * ACT_PITCH + s * 64 + g * 16);
    f32x4 acc[4] = {};
    {
      const int q4 = (lane >> 2) & 3, pp = lane & 3;
      const int f = ((q4 >> 1) & 1) | ((g & 1) << 1);
      LAS unsigned char* base = L + L_WQ + (8 * g + q4) * 128 + pp * 8;
#pragma unroll
      for (int s = 0; s < 4; ++s) {
        if (s == 0) asm volatile("s_waitcnt vmcnt(12)" ::: "memory");
        if (s == 1) asm volatile("s_waitcnt vmcnt(8)" ::: "memory");
        if (s == 2) asm volatile("s_waitcnt vmcnt(4)" ::: "memory");
        if (s == 3) { asm volatile("s_waitcnt vmcnt(0)" ::: "memory"); STAMP(2); }
#pragma unroll
        for (int t = 0; t < 4; ++t) {
          LAS unsigned char* q = base + s * 4096 + ((t ^ f) * 32);
          acc[t] = mfma(af[s], tr_frag(q, q + 512), acc[t]);
        }
      }
    }
    {
      const float s0 = (g & 1) ? acc[1][0] : acc[0][0], s1 = (g & 1) ? acc[1][1] : acc[0][1];
      const float s2 = (g & 1) ? acc[3][0] : acc[2][0], s3 = (g & 1) ? acc[3][1] : acc[2][1];
      const float q0 = (g & 2) ? s2 : s0, q1 = (g & 2) ? s3 : s1;
      const unsigned off = WS_QG + (((kvh * 4 + rg) * 2) * 512 + cg * 64 + lane) * 8;
      __builtin_amdgcn_raw_buffer_store_b64(v2u{__float_as_uint(q0), tag}, rs, off, 0, SC1);
      __builtin_amdgcn_raw_buffer_store_b64(v2u{__float_as_uint(q1), tag}, rs, off + 4096, 0, SC1);
    }
    STAMP(3);
    RBAR();
    {
      const unsigned off = WS_QG + ((kvh * 4 * 2 + b) * 512 + 2 * lane) * 8;
      v4u d[16];
#pragma unroll
      for (int i = 0; i < 16; ++i) d[i] = ldg(rs, off + (i >> 2) * 8192 + (i & 3) * 1024);
      RBAR();
      for (unsigned spin = 0; spin < SPIN_CAP; ++spin) {
        bool ok = true;
#pragma unroll
        for (int i = 0; i < 16; ++i) ok &= (d[i].y == tag) & (d[i].w == tag);
        if (__all(ok)) break;
        CBAR();
#pragma unroll
        for (int i = 0; i < 16; ++i) d[i] = ldg(rs, off + (i >> 2) * 8192 + (i & 3) * 1024);
      }
#pragma unroll
      for (int hh = 0; hh < 4; ++hh) {
        const float lo = (__uint_as_float(d[hh].x) + __uint_as_float(d[4 + hh].x)) + (__uint_as_float(d[8 + hh].x) + __uint_as_float(d[12 + hh].x));
        const float hi = (__uint_as_float(d[hh].z) + __uint_as_float(d[4 + hh].z)) + (__uint_as_float(d[8 + hh].z) + __uint_as_float(d[12 + hh].z));
        *(LAS unsigned*)(L + L_QS + hh * QS_PITCH + lane * 4) = pk_bf16(lo, hi);
      }
    }
    STAMP(4);
    BAR();
    RBAR();
    {
      const float mw = *(LAS float*)(L + L_MX + g * 32 + (c & 7) * 4), lw = *(LAS float*)(L + L_LS + g * 32 + (c & 7) * 4);
      float M = fmaxf(mw, dpp<XOR1>(mw)); M = fmaxf(M, dpp<XOR2>(M)); M = fmaxf(M, dpp<HMIR>(M));
      float S = lw * __builtin_amdgcn_exp2f(mw - M);
      S += dpp<XOR1>(S); S += dpp<XOR2>(S); S += dpp<HMIR>(S);
      if (c == 0)
        __builtin_amdgcn_raw_buffer_store_b128(v4u{__float_as_uint(M), tag, __float_as_uint(S), tag}, rs,
                                               WS_PA + ((b * 32 + kvh * 4 + g) * 16 + sp) * 640 + 512, 0, SC1);
    }
    if (j < 16) {
      const int h = kvh * 4 + (j >> 2), cq = j & 3;
      const unsigned v0 = g * 1024 + (((c >> 1) ^ g) * 32) + (c & 1) * 16;
      const u16* src = p.wo + (size_t)(h * 128) * 512 + cq * 128;
#pragma unroll
      for (int i = 0; i < 32; ++i) dma16(src + (size_t)i * 4 * 512, (i & 2) ? (v0 ^ 128u) : v0, LB + L_W + i * 1024);
    } else if (j < 23) {
      const int f = kvh * 7 + (j - 16);
      const int r0 = lane >> 2, pos = lane & 3;
      const unsigned v0 = r0 * (FF * 2) + (((pos >> 1) ^ ((r0 >> 3) & 1)) * 32) + (pos & 1) * 16;
      const u16* src = p.w1 + f * 32;
#pragma unroll
      for (int i = 0; i < 32; ++i) dma16(src + (size_t)i * 16 * FF, v0, LB + L_W + i * 1024);
      const u16* src2 = p.w2 + (size_t)(f * 32) * 512;
#pragma unroll
      for (int i = 0; i < 32; ++i)
        dma16(src2 + (size_t)i * 512, (unsigned)(lane * 16) ^ (unsigned)(((i & 3) | (((i >> 3) & 1) << 2)) * 32), LB + L_W + 32768 + i * 1024);
    }
  } else {
    const int w = wave - 1, b = j >> 4, sp = j & 15;
    if (w == 7 && j < CODE_KB) {
      asm volatile("s_mov_b32 m0, %2\n\ts_nop 0\n\tglobal_load_lds_dwordx4 %0, %1" ::"v"(j * 1024 + lane * 16), "s"(pc & ~255ull), "s"(LB + L_PF) : "memory");
    }
    const size_t base = ((size_t)(b * 8 + kvh) * SK + (SK - WIN) + sp * 128 + w * 16) * 128;
    const u16* kp = p.kc + base + c * 128 + g * 8;
    const u16* vp = p.vc + base + (4 * g) * 128 + c * 8;
    v4u kf[4], vf[4];
    RBAR();
#pragma unroll
    for (int s = 0; s < 4; ++s) kf[s] = LDNT(kp + s * 32);
    RBAR();
    vf[0] = LDNT(vp); vf[1] = LDNT(vp + 128); vf[2] = LDNT(vp + 256);
    asm volatile("" : "+v"(kf[0]), "+v"(kf[1]), "+v"(kf[2]), "+v"(kf[3])::"memory");
    STAMP(1);
    RBAR();
    RBAR();
    vf[3] = LDNT(vp + 384);
    const unsigned mk0 = (c & 2) ? 0u : ((c & 1) ? 0xffff0000u : 0x0000ffffu), mk1 = (c & 2) ? ((c & 1) ? 0xffff0000u : 0x0000ffffu) : 0u;
    RBAR();
    STAMP(2);
    tag = *(LAS unsigned*)(L + L_TAG);
    f32x4 sc = {};
    v4u qf[4];
#pragma unroll
    for (int s = 0; s < 4; ++s) qf[s] = *(LAS v4u*)(L + L_QS + (c >> 2) * QS_PITCH + s * 64 + g * 16);
    __builtin_amdgcn_sched_barrier(0);
#pragma unroll
    for (int s = 0; s < 4; ++s) sc = mfma(kf[s], qf[s], sc);
    float sv[4];
    {
      const unsigned r01 = pk_bf16(sc[0], sc[1]), r23 = pk_bf16(sc[2], sc[3]);
      const float k = 0.08838834764831845f;
      const unsigned t01 = pk_bf16(bf_lo(r01) * k, bf_hi(r01) * k), t23 = pk_bf16(bf_lo(r23) * k, bf_hi(r23) * k);
      sv[0] = bf_lo(t01); sv[1] = bf_hi(t01); sv[2] = bf_lo(t23); sv[3] = bf_hi(t23);
    }
    const float mw = x32_max(x16_max(fmaxf(fmaxf(sv[0], sv[1]), fmaxf(sv[2], sv[3]))));
    v2u pa[4];
    float lw = 0.f;
#pragma unroll
    for (int t = 0; t < 4; ++t) {
      const float e = __builtin_amdgcn_exp2f((sv[t] - mw) * LOG2E);
      lw += e;
      const unsigned pk = pk_bf16(e, e);
      pa[t] = v2u{pk & mk0, pk & mk1};
    }
    lw = x32_sum(x16_sum(lw));
    if ((lane & 51) == 0) {
      *(LAS float*)(L + L_MX + (c >> 2) * 32 + w * 4) = mw * LOG2E;
      *(LAS float*)(L + L_LS + (c >> 2) * 32 + w * 4) = lw;
    }
    STAMP(3);
    BAR();
    const float fac = __builtin_amdgcn_exp2f(*(LAS float*)(L + L_MX + g * 32 + w * 4) -
                                             max8(*(LAS f32x4*)(L + L_MX + g * 32), *(LAS f32x4*)(L + L_MX + g * 32 + 16)));
    f32x4 olo = {}, ohi = {};
#pragma unroll
    for (int t = 0; t < 4; ++t) { olo = mfma16(pa[t], v2u{vf[t].x, vf[t].y}, olo); ohi = mfma16(pa[t], v2u{vf[t].z, vf[t].w}, ohi); }
    *(LAS f32x4*)(L + L_RED + w * RED_PITCH + g * 512 + c * 16) = olo * fac;
    *(LAS f32x4*)(L + L_RED + w * RED_PITCH + g * 512 + 256 + c * 16) = ohi * fac;
    STAMP(4);
    {
      CBAR();
      unsigned cnt = 0;
      if (lane == 0) cnt = __hip_atomic_fetch_add((LAS unsigned*)(L + L_TAG + 4), 1u, __ATOMIC_RELAXED, __HIP_MEMORY_SCOPE_WORKGROUP) + 1u;
      cnt = __builtin_amdgcn_readfirstlane(cnt);
      while (cnt < 8u) cnt = __builtin_amdgcn_readfirstlane(*(volatile LAS unsigned*)(L + L_TAG + 4));
      CBAR();
    }
    {
      const int hh = w >> 1, pr = (w & 1) * 32 + (lane >> 1), half = lane & 1;
      LAS unsigned char* src = L + L_RED + (4 * half) * RED_PITCH + hh * 512 + ((pr >> 1) & 1) * 256 + (pr >> 2) * 16 + (pr & 1) * 8;
      f32x2 a = (*(LAS f32x2*)(src) + *(LAS f32x2*)(src + RED_PITCH)) + (*(LAS f32x2*)(src + 2 * RED_PITCH) + *(LAS f32x2*)(src + 3 * RED_PITCH));
      a[0] += dpp<XOR1>(a[0]); a[1] += dpp<XOR1>(a[1]);
      if (half == 0)
        __builtin_amdgcn_raw_buffer_store_b64(v2u{pk_bf16(a[0], a[1]), tag}, rs, WS_PA + ((b * 32 + kvh * 4 + hh) * 16 + sp) * 640 + pr * 8, 0, SC1);
    }
    STAMP(5);
  }

  if (j < 16) {
    const int h = kvh * 4 + (j >> 2), cq = j & 3;
    if (wave == 0) {
      const unsigned off = WS_PA + (((g & 1) * 32 + h) * 16 + c) * 640 + 512;
      v4u d;
      for (unsigned spin = 0; spin < SPIN_CAP; ++spin) {
        CBAR();
        d = ldg(rs, off);
        if (__all((d.y == tag) & (d.w == tag))) break;
      }
      const float m = __uint_as_float(d.x);
      float M = fmaxf(m, dpp<XOR1>(m)); M = fmaxf(M, dpp<XOR2>(M)); M = fmaxf(M, dpp<HMIR>(M)); M = fmaxf(M, dpp<ROR8>(M));
      const float wv = __builtin_amdgcn_exp2f(m - M);
      float S = wv * __uint_as_float(d.z);
      S += dpp<XOR1>(S); S += dpp<XOR2>(S); S += dpp<HMIR>(S); S += dpp<ROR8>(S);
      if (g < 2) *(LAS float*)(L + L_WN + lane * 4) = wv / S;
      asm volatile("s_waitcnt vmcnt(0)" ::: "memory");
      STAMP(6);
      BAR();
      STAMP_FLUSH();
      return;
    }
    const int w = wave - 1;
    const int b = w >> 2, dq = w & 3, s8 = lane & 7, xx = lane >> 3;
    const unsigned off = WS_PA + ((b * 32 + h) * 16 + s8) * 640 + dq * 128 + xx * 16;
    v4u d0 = ldg(rs, off), d1 = ldg(rs, off + 8 * 640);
    RBAR();
    v4u wf[4];
    {
      const int q4 = (lane >> 2) & 3, pp = lane & 3;
      const int f = q4 | ((g & 1) << 2);
#pragma unroll
      for (int s = 0; s < 4; ++s) {
        LAS unsigned char* base = L + L_W + (32 * s + 8 * g + q4) * 256 + ((w ^ f) * 32) + pp * 8;
        wf[s] = tr_frag(base, base + 1024);
      }
    }
    float w0 = *(LAS float*)(L + L_WN + b * 64 + s8 * 4), w1 = *(LAS float*)(L + L_WN + b * 64 + 32 + s8 * 4);
    asm volatile("" : "+v"(wf[0]), "+v"(wf[1]), "+v"(wf[2]), "+v"(wf[3]), "+v"(w0), "+v"(w1));
    if (!__all((d0.y == tag) & (d0.w == tag) & (d1.y == tag) & (d1.w == tag))) {
      v4u d[2];
      poll<2, 8 * 640>(rs, off, tag, d);
      d0 = d[0]; d1 = d[1];
    }
    STAMP(6);
    {
      const float v0 = w0 * bf_lo(d0.x) + w1 * bf_lo(d1.x), v1 = w0 * bf_hi(d0.x) + w1 * bf_hi(d1.x);
      const float v2 = w0 * bf_lo(d0.z) + w1 * bf_lo(d1.z), v3 = w0 * bf_hi(d0.z) + w1 * bf_hi(d1.z);
      const float a0 = v0 + dpp<HMIR>(v0), a1 = v1 + dpp<HMIR>(v1), a2 = v2 + dpp<HMIR>(v2), a3 = v3 + dpp<HMIR>(v3);
      float u0 = (s8 & 4) ? a2 : a0, u1 = (s8 & 4) ? a3 : a1;
      u0 += dpp<XOR2>(u0); u1 += dpp<XOR2>(u1);
      u0 += dpp<XOR1>(u0); u1 += dpp<XOR1>(u1);
      if ((s8 & 3) == 0) *(LAS unsigned*)(L + L_ACT + b * ACT_PITCH + (dq * 32 + xx * 4 + (s8 >> 2) * 2) * 2) = pk_bf16(u0, u1);
    }
    BAR();
    f32x4 acc = {};
    v4u of[4];
#pragma unroll
    for (int s = 0; s < 4; ++s) of[s] = *(LAS v4u*)(L + L_ACT + (c & 1) * ACT_PITCH + s * 64 + g * 16);
    __builtin_amdgcn_sched_barrier(0);
#pragma unroll
    for (int s = 0; s < 4; ++s) acc = mfma(of[s], wf[s], acc);
    if (g == 0)
      __builtin_amdgcn_raw_buffer_store_b128(v4u{__float_as_uint(acc[0]), tag, __float_as_uint(acc[1]), tag}, rs,
                                             WS_PB + ((cq * 8 + w) * 32 + h) * 256 + c * 16, 0, SC1);
    STAMP(7);
  } else if (j < 23) {
    const int f = kvh * 7 + (j - 16);
    if (wave == 0) {
      asm volatile("s_waitcnt vmcnt(0)" ::: "memory");
      STAMP(6);
      RBAR();
      RBAR();
      const unsigned off = WS_HG + lane * 64;
      v4u d[4];
      poll<4, 16>(rs, off, tag, d);
      STAMP(7);
      {
        LAS unsigned char* dst = L + L_H + (lane & 1) * H_PITCH + (lane >> 1) * 32;
        *(LAS v4u*)dst = v4u{d[0].x, d[0].z, d[1].x, d[1].z};
        *(LAS v4u*)(dst + 16) = v4u{d[2].x, d[2].z, d[3].x, d[3].z};
      }
      BAR();
      float s0 = 0.f, s1 = 0.f, q0 = 0.f, q1 = 0.f;
#pragma unroll
      for (int i = 0; i < 4; ++i) {
        s0 = dot2(d[i].x, 0x3f803f80u, s0); s1 = dot2(d[i].z, 0x3f803f80u, s1);
        q0 = dot2(d[i].x, d[i].x, q0); q1 = dot2(d[i].z, d[i].z, q1);
      }
      const float mean = wave_sum_par(s0 + s1) * (1.0f / 512.0f);
      const float ex2 = wave_sum_par(q0 + q1) * (1.0f / 512.0f);
      const float var = fmaxf(fmaf(-mean, mean, ex2), 0.f);
      const float inv = __builtin_amdgcn_rcpf(rbf(__builtin_amdgcn_sqrtf(rbf(rbf(var) + 1e-5f))));
      if (lane < 2) *(LAS f32x2*)(L + L_WN + lane * 8) = f32x2{rbf(mean), inv};
      BAR();
      STAMP(8);
      STAMP_FLUSH();
      return;
    }
    const int w = wave - 1;
    const float bias1 = bf_lo(p.b1[f * 32 + (lane & 31)]);
    RBAR();
    v4u w1f[4], w2f[4];
    {
      const int q4 = (lane >> 2) & 3, pp = lane & 3;
#pragma unroll
      for (int s = 0; s < 4; ++s) {
        LAS unsigned char* base = L + L_W + (64 * w + 32 * (s >> 1) + 8 * g + q4) * 64 + (((s & 1) ^ (g & 1)) * 32) + pp * 8;
        w1f[s] = tr_frag(base, base + 256);
      }
      const int fz = q4 | ((g & 1) << 2);
#pragma unroll
      for (int t = 0; t < 4; ++t) {
        LAS unsigned char* base = L + L_W + 32768 + (8 * g + q4) * 1024 + (((4 * w + t) ^ fz) * 32) + pp * 8;
        w2f[t] = tr_frag(base, base + 4096);
      }
    }
    const f32x4 z4 = {};
    LAS float* zdst = (LAS float*)(L + L_ZP + ((g & 1) * 32 + (g >> 1) * 16 + c) * 48 + w * 4);
    float csum;
    {
      const v4u ones = {0x3f803f80u, 0x3f803f80u, 0x3f803f80u, 0x3f803f80u};
      const f32x4 c0 = mfma(ones, w1f[2], mfma(ones, w1f[0], z4)), c1 = mfma(ones, w1f[3], mfma(ones, w1f[1], z4));
      *zdst = (g & 2) ? c1[0] : c0[0];
      BAR();
      const f32x4 a = *(LAS f32x4*)(L + L_ZP + lane * 48), bq = *(LAS f32x4*)(L + L_ZP + lane * 48 + 16);
      csum = ((a[0] + a[1]) + (a[2] + a[3])) + ((bq[0] + bq[1]) + (bq[2] + bq[3]));
      asm volatile("" : "+v"(csum), "+v"(w2f[0]), "+v"(w2f[1]), "+v"(w2f[2]), "+v"(w2f[3]));
    }
    BAR();
    STAMP(7);
    {
      const v4u a0 = *(LAS v4u*)(L + L_H + (c & 1) * H_PITCH + w * 128 + g * 16), a1 = *(LAS v4u*)(L + L_H + (c & 1) * H_PITCH + w * 128 + 64 + g * 16);
      const f32x4 z0 = mfma(a1, w1f[2], mfma(a0, w1f[0], z4)), z1 = mfma(a1, w1f[3], mfma(a0, w1f[1], z4));
      const float t0 = (g & 1) ? z0[1] : z0[0], t1 = (g & 1) ? z1[1] : z1[0];
      *zdst = (g & 2) ? t1 : t0;
    }
    BAR();
    {
      const f32x4 a = *(LAS f32x4*)(L + L_ZP + lane * 48), bq = *(LAS f32x4*)(L + L_ZP + lane * 48 + 16);
      const f32x2 st = *(LAS f32x2*)(L + L_WN + (lane >> 5) * 8);
      const float zr = ((a[0] + a[1]) + (a[2] + a[3])) + ((bq[0] + bq[1]) + (bq[2] + bq[3]));
      const float z = rbf(rbf(fmaf(-st[0], csum, zr) * st[1]) + bias1);
      const float t = fmaf(z * z, 0.044715f * 0.7978845608028654f * 2.f * LOG2E, 0.7978845608028654f * 2.f * LOG2E);
      const float e = __builtin_amdgcn_exp2f(z * t);
      const float gl = fmaf(-z, __builtin_amdgcn_rcpf(e + 1.0f), z);
      *(LAS u16*)(L + L_GA + w * 160 + (lane >> 5) * 80 + (lane & 31) * 2) = (u16)pk_bf16(gl, gl);
    }
    CBAR();
    const v4u ga = *(LAS v4u*)(L + L_GA + w * 160 + (c & 1) * 80 + g * 16);
    const f32x4 y0 = mfma(ga, w2f[0], z4), y1 = mfma(ga, w2f[1], z4), y2 = mfma(ga, w2f[2], z4), y3 = mfma(ga, w2f[3], z4);
    const float s0 = (g & 1) ? y1[0] : y0[0], s1 = (g & 1) ? y1[1] : y0[1], s2 = (g & 1) ? y3[0] : y2[0], s3 = (g & 1) ? y3[1] : y2[1];
    __builtin_amdgcn_raw_buffer_store_b128(v4u{__float_as_uint((g & 2) ? s2 : s0), tag, __float_as_uint((g & 2) ? s3 : s1), tag}, rs,
                                           WS_PD + ((2 * (4 * w + g) + (c >> 3)) * 56 + f) * 128 + (c & 7) * 16, 0, SC1);
    STAMP(8);
  } else if (j < 31) {
    if (wave == 0 && j < 27) {
      const int r = kvh * 4 + (j - 23);
      const float x0 = bf_lo(p.x[r * 16 + c]), x1 = bf_lo(p.x[512 + r * 16 + c]);
      const unsigned off = WS_PB + (r * 32 + g) * 256 + c * 16;
      v4u d[8];
      poll<8, 1024>(rs, off, tag, d);
      STAMP(6);
#define F_(v) __uint_as_float(v)
      float a0 = ((F_(d[0].x) + F_(d[1].x)) + (F_(d[2].x) + F_(d[3].x))) + ((F_(d[4].x) + F_(d[5].x)) + (F_(d[6].x) + F_(d[7].x)));
      float a1 = ((F_(d[0].z) + F_(d[1].z)) + (F_(d[2].z) + F_(d[3].z))) + ((F_(d[4].z) + F_(d[5].z)) + (F_(d[6].z) + F_(d[7].z)));
      a0 = x32_sum(x16_sum(a0)); a1 = x32_sum(x16_sum(a1));
      const unsigned ar = pk_bf16(a0, a1);
      const float h0 = x0 + bf_lo(ar), h1 = x1 + bf_hi(ar);
      const float n0 = dpp<XOR1>(h0), n1 = dpp<XOR1>(h1);
      const unsigned pk = pk_bf16((c & 1) ? n1 : h0, (c & 1) ? h1 : n0);
      if (g == 0) __builtin_amdgcn_raw_buffer_store_b64(v2u{pk, tag}, rs, WS_HG + r * 128 + ((c & 1) * 8 + (c >> 1)) * 8, 0, SC1);
      if (r == 0 && lane == 0) __builtin_amdgcn_raw_buffer_store_b32(tag, rs, WS_EPOCH, 0, SC1);
      STAMP(7);
    } else if (wave == 1) {
      const int r2 = kvh * 8 + (j - 23), n = lane & 7, fg = lane >> 3;
      const float bias2 = bf_lo(p.b2[r2 * 8 + n]);
      v2u hg0, hg1;
      const unsigned hoff = WS_HG + (r2 >> 1) * 128 + ((r2 & 1) * 4 + (n >> 1)) * 8;
      for (unsigned spin = 0; spin < SPIN_CAP; ++spin) {
        CBAR();
        hg0 = __builtin_amdgcn_raw_buffer_load_b64(rs, hoff, 0, SC1);
        hg1 = __builtin_amdgcn_raw_buffer_load_b64(rs, hoff + 64, 0, SC1);
        if (__all((hg0.y == tag) & (hg1.y == tag))) break;
        __builtin_amdgcn_s_sleep(2);
      }
      STAMP(6);
      const float h0 = (n & 1) ? bf_hi(hg0.x) : bf_lo(hg0.x), h1 = (n & 1) ? bf_hi(hg1.x) : bf_lo(hg1.x);
      const unsigned off = WS_PD + (r2 * 56 + fg) * 128 + n * 16;
      v4u d[7];
      poll<7, 1024>(rs, off, tag, d);
      STAMP(7);
      float a0 = ((F_(d[0].x) + F_(d[1].x)) + (F_(d[2].x) + F_(d[3].x))) + ((F_(d[4].x) + F_(d[5].x)) + F_(d[6].x));
      float a1 = ((F_(d[0].z) + F_(d[1].z)) + (F_(d[2].z) + F_(d[3].z))) + ((F_(d[4].z) + F_(d[5].z)) + F_(d[6].z));
      a0 += dpp<ROR8>(a0); a1 += dpp<ROR8>(a1);
      a0 = x32_sum(x16_sum(a0)); a1 = x32_sum(x16_sum(a1));
      const unsigned ar = pk_bf16(a0, a1);
      const unsigned yr = pk_bf16(bf_lo(ar) + bias2, bf_hi(ar) + bias2);
      const float o0 = h0 + bf_lo(yr), o1 = h1 + bf_hi(yr);
      const float n0 = dpp<XOR1>(o0), n1 = dpp<XOR1>(o1);
      const unsigned pk = pk_bf16((n & 1) ? n1 : o0, (n & 1) ? o1 : n0);
      if (lane < 8) *(unsigned*)(p.out + (n & 1) * 512 + r2 * 8 + (n & 6)) = pk;
      STAMP(8);
    }
  }
  STAMP_FLUSH();
}

__global__ void spin_k(int us10) { extern __shared__ unsigned char sl[]; unsigned long long t0; asm volatile("s_memrealtime %0\n\ts_waitcnt lgkmcnt(0)" : "=s"(t0)); while ((unsigned)__builtin_amdgcn_s_memrealtime() - (unsigned)t0 < 2000u) __builtin_amdgcn_s_sleep(1); }
}

extern "C" void kernel_launch(void* const* d_in, const int* in_sizes, int n_in,
                              void* d_out, int out_size, void* d_ws, size_t ws_size,
                              hipStream_t stream) {
  Params p;
  p.x = (const u16*)d_in[0]; p.kc = (const u16*)d_in[1]; p.vc = (const u16*)d_in[2];
  p.wq = (const u16*)d_in[3]; p.wo = (const u16*)d_in[4];
  p.w1 = (const u16*)d_in[6]; p.b1 = (const u16*)d_in[7]; p.w2 = (const u16*)d_in[8]; p.b2 = (const u16*)d_in[9];
  p.out = (u16*)d_out; p.ws = (unsigned char*)d_ws;
  (void)hipFuncSetAttribute((const void*)fused_decode, hipFuncAttributeMaxDynamicSharedMemorySize, LDS_BYTES);
  hipLaunchKernelGGL(fused_decode, dim3(256), dim3(NTHR), LDS_BYTES, stream, p);
  (void)hipFuncSetAttribute((const void*)spin_k, hipFuncAttributeMaxDynamicSharedMemorySize, 98304);
  hipLaunchKernelGGL(spin_k, dim3(256), dim3(576), 90000, stream, 0);
}
```

```cpp
#include <hip/hip_runtime.h>
#include <hip/hip_bf16.h>
#include <stdint.h>

typedef unsigned short u16;
typedef unsigned long long u64;
typedef unsigned v4u __attribute__((ext_vector_type(4)));
typedef unsigned v2u __attribute__((ext_vector_type(2)));
typedef float f32x4 __attribute__((ext_vector_type(4)));
typedef float f32x2 __attribute__((ext_vector_type(2)));
typedef short bf16x8 __attribute__((ext_vector_type(8)));
typedef short bf16x4 __attribute__((ext_vector_type(4)));
typedef __bf16 bf16x2 __attribute__((ext_vector_type(2)));
typedef short v4i16 __attribute__((ext_vector_type(4)));
#define LAS __attribute__((address_space(3)))
#define GAS __attribute__((address_space(1)))

namespace {

constexpr int SK = 8192, WIN = 2048, FF = 1792;
constexpr int NTHR = 320;
#define QDELAY 22
#define PACE 1
#define WN1 32
#ifndef NV0
#define NV0 0
#endif
#define QDELAY2 0
#define WN1F 16
#ifndef NV1
#define NV1 2
#endif
#define KS1 4
#define KW1 3
#define KS2 8
#define KW2 0
#define RDDELAY 0
#define HOMING 1
#define NEARP(ws) near_parity(ws)
#define FBOTH 0
#define VW1 5
#define DEPH 1
#define CODE_KB 13
constexpr unsigned SPIN_CAP = 1u << 18;
constexpr float LOG2E = 1.4426950408889634f;

constexpr unsigned WS_EPOCH = 0;
constexpr unsigned WS_CLAIM = 1024;
constexpr unsigned WS_CAL = 16384;
constexpr unsigned WS_HG = WS_CAL + 65536;
constexpr unsigned WS_PB = WS_HG + 3 * 16384;
constexpr unsigned WS_PD = WS_PB + 8 * 16384;
constexpr unsigned WS_PA = WS_PD + 32 * 8192;
constexpr unsigned WS_QG = WS_PA + 8 * 10 * 16384;
constexpr unsigned WS_END = WS_QG + 8 * 4 * 2 * 512 * 8;
__host__ __device__ constexpr unsigned pa_data(unsigned kvh, unsigned hh, unsigned b) { return WS_PA + (kvh * 10 + hh * 2 + b) * 16384; }
__host__ __device__ constexpr unsigned pa_ml(unsigned kvh, unsigned hh, unsigned b) { return WS_PA + (kvh * 10 + 8 + (hh >> 1)) * 16384 + ((hh & 1) * 2 + b) * 2048; }
constexpr unsigned WS_STAMP = 4u << 20;
__host__ __device__ constexpr unsigned pb_inbox(unsigned r) { return WS_PB + (r >> 2) * 16384 + (r & 1) * 8192 + ((r >> 1) & 1) * 4096; }

constexpr int L_WQ = 0;
constexpr int RED_PITCH = 2048;
constexpr int L_RED = 0;
constexpr int L_W = 17408;
constexpr int L_K = L_W + 32768;
constexpr int L_ACT = L_W + 65536;
constexpr int ACT_PITCH = 256 + 64;
constexpr int L_QS = L_ACT + 2 * ACT_PITCH;
constexpr int QS_PITCH = 256 + 64;
constexpr int L_MX = L_QS + 4 * QS_PITCH;
constexpr int L_LS = L_MX + 128;
constexpr int L_WN = L_LS + 128;
constexpr int L_ZP = L_WN + 128;
constexpr int L_GA = L_ZP + 64 * 48;
constexpr int L_H = L_GA + 8 * 160;
constexpr int H_PITCH = 1024 + 64;
constexpr int L_TAG = L_H + 2 * H_PITCH;
constexpr int L_PF = L_TAG + 64;
constexpr int LDS_BYTES = L_PF + 1024;
static_assert(8 * RED_PITCH <= L_W, "lds map");
static_assert(LDS_BYTES > 81920, "one workgroup per CU");

struct Params {
  const u16* x; const u16* kc; const u16* vc; const u16* wq; const u16* wo;
  const u16* w1; const u16* b1; const u16* w2; const u16* b2;
  u16* out; unsigned char* ws;
};

#define F_(v) __uint_as_float(v)
#define U_(v) __float_as_uint(v)
__device__ __forceinline__ float bf_lo(unsigned d) { return F_(d << 16); }
__device__ __forceinline__ float bf_hi(unsigned d) { return F_(d & 0xffff0000u); }
__device__ __forceinline__ unsigned pk_bf16(float lo, float hi) {
  return __builtin_bit_cast(unsigned, __builtin_convertvector(f32x2{lo, hi}, bf16x2));
}
__device__ __forceinline__ float rbf(float f) { return bf_lo(pk_bf16(f, f)); }
__device__ __forceinline__ f32x4 mfma(v4u a, v4u b, f32x4 c) {
  return __builtin_amdgcn_mfma_f32_16x16x32_bf16(__builtin_bit_cast(bf16x8, a), __builtin_bit_cast(bf16x8, b), c, 0, 0, 0);
}
__device__ __forceinline__ f32x4 mfma16(v2u a, v2u b, f32x4 c) {
  return __builtin_amdgcn_mfma_f32_16x16x16bf16_1k(__builtin_bit_cast(bf16x4, a), __builtin_bit_cast(bf16x4, b), c, 0, 0, 0);
}
__device__ __forceinline__ float dot2(unsigned a, unsigned b, float acc) {
  return __builtin_amdgcn_fdot2_f32_bf16(__builtin_bit_cast(bf16x2, a), __builtin_bit_cast(bf16x2, b), acc, false);
}
template <int CTRL> __device__ __forceinline__ float dpp(float x) {
  return __builtin_bit_cast(float, __builtin_amdgcn_mov_dpp(__builtin_bit_cast(int, x), CTRL, 0xf, 0xf, true));
}
constexpr int XOR1 = 0xB1, XOR2 = 0x4E, HMIR = 0x141, ROR8 = 0x128;
__device__ __forceinline__ float x16_sum(float x) {
  auto s = __builtin_amdgcn_permlane16_swap(U_(x), U_(x), false, false);
  return F_(s[0]) + F_(s[1]);
}
__device__ __forceinline__ float x32_sum(float x) {
  auto s = __builtin_amdgcn_permlane32_swap(U_(x), U_(x), false, false);
  return F_(s[0]) + F_(s[1]);
}
__device__ __forceinline__ float x16_sum2(float a, float b) {
  auto s = __builtin_amdgcn_permlane16_swap(U_(a), U_(b), false, false);
  return F_(s[0]) + F_(s[1]);
}
__device__ __forceinline__ float x32_sum2(float a, float b) {
  auto s = __builtin_amdgcn_permlane32_swap(U_(a), U_(b), false, false);
  return F_(s[0]) + F_(s[1]);
}
__device__ __forceinline__ float vmax(float a, float b) { float r; asm("v_max_f32 %0, %1, %2" : "=v"(r) : "v"(a), "v"(b)); return r; }
__device__ __forceinline__ float vmax3(float a, float b, float c) { float r; asm("v_max3_f32 %0, %1, %2, %3" : "=v"(r) : "v"(a), "v"(b), "v"(c)); return r; }
__device__ __forceinline__ float xrows_max(float x) {
  auto s = __builtin_amdgcn_permlane16_swap(U_(x), U_(x), false, false);
  x = vmax(F_(s[0]), F_(s[1]));
  auto t = __builtin_amdgcn_permlane32_swap(U_(x), U_(x), false, false);
  return vmax(F_(t[0]), F_(t[1]));
}
#define BAR() asm volatile("s_waitcnt lgkmcnt(0)\n\ts_barrier" ::: "memory")
#define RBAR() asm volatile("s_barrier" ::: "memory")
#define CBAR() asm volatile("" ::: "memory")
#define PIN(x) asm volatile("" : "+v"(x))
#define LDNT(p) __builtin_nontemporal_load((const v4u*)(p))
#define SC1 16
#define LD(T, a) (*(LAS T*)(a))

__device__ __forceinline__ void dma16(const void* sbase, unsigned voff, unsigned ldsaddr) {
  asm volatile("s_mov_b32 m0, %2\n\ts_nop 0\n\tglobal_load_lds_dwordx4 %0, %1 nt" ::"v"(voff), "s"(sbase), "s"(ldsaddr) : "memory");
}
__device__ __forceinline__ v4u tr_frag(unsigned a0, unsigned a1) {
  v2u a = __builtin_bit_cast(v2u, __builtin_amdgcn_ds_read_tr16_b64_v4i16((LAS v4i16*)a0));
  v2u b = __builtin_bit_cast(v2u, __builtin_amdgcn_ds_read_tr16_b64_v4i16((LAS v4i16*)a1));
  return v4u{a.x, a.y, b.x, b.y};
}
__device__ __forceinline__ v4u ldg(__amdgpu_buffer_rsrc_t rs, unsigned off) { return __builtin_amdgcn_raw_buffer_load_b128(rs, off, 0, SC1); }
__device__ __forceinline__ void lds_arrive(unsigned addr) {
  asm volatile("s_mov_b64 exec, 1\n\tds_add_u32 %0, %1\n\ts_mov_b64 exec, -1" ::"v"(addr), "v"(1u) : "memory");
}

__device__ __forceinline__ void cal_issue(const unsigned char* ws) {
  const unsigned o = WS_CAL + (blockIdx.x >> 6) * 16384 + (blockIdx.x & 63) * 128;
  asm volatile("s_mov_b64 exec, 1\n\tglobal_store_dword %0, %0, %2 sc1\n\tglobal_store_dword %1, %0, %2 sc1\n\ts_mov_b64 exec, -1"
               ::"v"(o), "v"(o + 8192), "s"(ws) : "memory");
}
__device__ __forceinline__ unsigned cal_result() {
  u64 t1, t2;
  asm volatile("s_waitcnt vmcnt(1)\n\ts_memtime %0\n\ts_waitcnt vmcnt(0)\n\ts_memtime %1\n\ts_waitcnt lgkmcnt(0)" : "=&s"(t1), "=&s"(t2)::"memory");
  return (((unsigned)t2 - (unsigned)t1) > 100u ? 0u : 1u) ^ (HOMING == 2 ? 1u : 0u);
}
__device__ __forceinline__ unsigned claim_role(unsigned char* ws, unsigned role, unsigned tag) {
  unsigned old = 0;
  if ((threadIdx.x & 63) == 0) old = __hip_atomic_exchange((GAS unsigned*)(ws + WS_CLAIM + role * 128), tag, __ATOMIC_RELAXED, __HIP_MEMORY_SCOPE_AGENT);
  return __builtin_amdgcn_readfirstlane(old) == tag ? role ^ 1u : role;
}

template <int N, int STRIDE>
__device__ __forceinline__ void poll(__amdgpu_buffer_rsrc_t rs, unsigned off, unsigned tag, v4u (&d)[N]) {
#pragma unroll 1
  for (unsigned spin = 0; spin < SPIN_CAP; ++spin) {
    CBAR();
#pragma unroll
    for (int i = 0; i < N; ++i) d[i] = ldg(rs, off + i * STRIDE);
    unsigned bad = 0;
#pragma unroll
    for (int i = 0; i < N; ++i) bad |= (d[i].y ^ tag) | (d[i].w ^ tag);
    if (__builtin_amdgcn_ballot_w64(bad != 0u) == 0ull) break;
  }
}

__device__ __forceinline__ f32x2 wave_sum2(f32x2 x) {
  x += f32x2{dpp<XOR1>(x[0]), dpp<XOR1>(x[1])}; x += f32x2{dpp<XOR2>(x[0]), dpp<XOR2>(x[1])};
  x += f32x2{dpp<HMIR>(x[0]), dpp<HMIR>(x[1])}; x += f32x2{dpp<ROR8>(x[0]), dpp<ROR8>(x[1])};
  const float t = x32_sum(x16_sum2(x[0], x[1]));
  auto u = __builtin_amdgcn_permlane16_swap(U_(t), U_(t), false, false);
  return f32x2{F_(u[0]), F_(u[1])};
}
__device__ __forceinline__ void ln_stats(v4u a, v4u b, unsigned& mb, unsigned& den) {
  f32x2 v[8];
#pragma unroll
  for (int i = 0; i < 4; ++i) { v[2 * i] = f32x2{bf_lo(a[i]), bf_lo(b[i])}; v[2 * i + 1] = f32x2{bf_hi(a[i]), bf_hi(b[i])}; }
  const f32x2 mean = wave_sum2(((v[0] + v[1]) + (v[2] + v[3])) + ((v[4] + v[5]) + (v[6] + v[7]))) * (1.0f / 512.0f);
#pragma unroll
  for (int i = 0; i < 8; ++i) { v[i] -= mean; v[i] *= v[i]; }
  const f32x2 var = wave_sum2(((v[0] + v[1]) + (v[2] + v[3])) + ((v[4] + v[5]) + (v[6] + v[7]))) * (1.0f / 512.0f);
  mb = pk_bf16(mean[0], mean[1]);
  const unsigned w = pk_bf16(var[0], var[1]);
  const unsigned e = pk_bf16(bf_lo(w) + 1e-5f, bf_hi(w) + 1e-5f);
  den = pk_bf16(__builtin_amdgcn_sqrtf(bf_lo(e)), __builtin_amdgcn_sqrtf(bf_hi(e)));
}

#define STAMP(i) do {} while (0)
#define STAMP_DECL
#define STAMP_FLUSH() do {} while (0)
#define STAMP2(i) do {} while (0)

__global__ void __launch_bounds__(NTHR, 1) fused_decode(Params p) {
  extern __shared__ __attribute__((aligned(16))) unsigned char lds_raw[];
  u64 pc;
  asm volatile("s_getpc_b64 %0" : "=s"(pc));
  const unsigned LB = (unsigned)(size_t)(LAS unsigned char*)lds_raw;
  const int tid = threadIdx.x, lane = tid & 63, wave = __builtin_amdgcn_readfirstlane(tid >> 6);
  const int kvh = blockIdx.x & 7, j = blockIdx.x >> 3;
  const int g = lane >> 4, c = lane & 15;
  STAMP_DECL;
  STAMP(0);
  if (wave == 4 && j < CODE_KB)
    asm volatile("s_mov_b32 m0, %2\n\ts_nop 0\n\tglobal_load_lds_dwordx4 %0, %1" ::"v"(j * 1024 + lane * 16), "s"(pc & ~255ull), "s"(LB + L_PF) : "memory");
  const __amdgpu_buffer_rsrc_t rs = __builtin_amdgcn_make_buffer_rsrc((void*)p.ws, 0, WS_END, 0x00020000);
  const int b = j >> 4, sp = j & 15;
  unsigned tag, npo;

  const int rg = j >> 3, cg = j & 7;
  const int dph = DEPH ? (j + kvh * 5) : 0;
  unsigned ep; v4u x0, x1; v2u xs;
  if (wave == 4) cal_issue(p.ws);
  RBAR();
  if (wave == 4) { const unsigned np0 = cal_result(); if (lane == 0) LD(unsigned, LB + L_TAG + 40) = np0; }
  if (wave == 0) {
    asm volatile("global_load_dword %0, %1, %2 sc1" : "=v"(ep) : "v"(WS_EPOCH), "s"(p.ws) : "memory");
    asm volatile("global_load_dwordx4 %0, %1, %2" : "=v"(x0) : "v"(lane * 16), "s"(p.x) : "memory");
    asm volatile("global_load_dwordx4 %0, %1, %2 offset:1024" : "=v"(x1) : "v"(lane * 16), "s"(p.x) : "memory");
    asm volatile("global_load_dwordx2 %0, %1, %2" : "=v"(xs) : "v"((lane >> 5) * 1024 + rg * 256 + (lane & 31) * 8), "s"(p.x) : "memory");
    const int r0 = lane >> 3, pos = lane & 7;
    const unsigned v0 = r0 * 8192 + (((pos >> 1) ^ ((r0 >> 1) & 1)) * 32) + (pos & 1) * 16;
    const u16* src = p.wq + (size_t)(rg * 128) * 4096 + kvh * 512 + cg * 64;
    const unsigned v1 = (v0 ^ 64u) + 65536u, va = (dph & 8) ? v1 : v0, vb = (dph & 8) ? v0 : v1, la = (dph & 8) ? 1024u : 0u;
#pragma unroll 1
    for (int i = 0; i < 8; ++i) {
      const int ii = i ^ (dph & 7);
      dma16(src + (size_t)ii * 16 * 4096, va, LB + L_WQ + ii * 2048 + la);
      dma16(src + (size_t)ii * 16 * 4096, vb, LB + L_WQ + ii * 2048 + (la ^ 1024));
    }
  }
  const size_t kvbase = ((size_t)(b * 8 + kvh) * SK + (SK - WIN) + sp * 128) * 128;
  const unsigned char* kp = (const unsigned char*)(p.kc + kvbase);
  const unsigned char* vp = (const unsigned char*)(p.vc + kvbase);
  const int pk0 = (((wave - 1) * 8) ^ dph) & 31;
  unsigned ko = g * 256 + ((c ^ (4 * g)) * 16);
  PIN(ko);
  BAR();
  int wave2 = wave;
  asm volatile("" : "+s"(wave2));

  if (wave2 == 0) {
    if (lane < 8) LD(unsigned, LB + L_TAG + 4 + lane * 4) = 0u;
    const unsigned np0 = LD(unsigned, LB + L_TAG + 40);
    const int q4 = (lane >> 2) & 3, pp = lane & 3;
    unsigned tra = LB + L_WQ + (8 * g + q4) * 128 + pp * 8 + ((((q4 >> 1) & 1) | ((g & 1) << 1)) * 32) + ((dph >> 1) & 3) * 4096;
    unsigned qoff = WS_QG + (((kvh * 4 + rg) * 2) * 512 + cg * 64 + lane) * 8;
    unsigned goff = WS_QG + ((kvh * 4 * 2 + b) * 512 + 2 * lane) * 8;
    PIN(tra); PIN(qoff); PIN(goff);
    asm volatile("s_waitcnt vmcnt(16)" : "+v"(ep), "+v"(x0), "+v"(x1), "+v"(xs)::"memory");
    tag = __builtin_amdgcn_readfirstlane(ep) + 1u;
    STAMP2(1);
    v4u af[4];
    {
      unsigned mb2, den2;
      ln_stats(x0, x1, mb2, den2);
      const float mb = lane < 32 ? bf_lo(mb2) : bf_hi(mb2), den = lane < 32 ? bf_lo(den2) : bf_hi(den2);
      const float inv = __builtin_amdgcn_rcpf(den);
      const unsigned c0 = pk_bf16(bf_lo(xs.x) - mb, bf_hi(xs.x) - mb), c1 = pk_bf16(bf_lo(xs.y) - mb, bf_hi(xs.y) - mb);
      float a[4] = {bf_lo(c0), bf_hi(c0), bf_lo(c1), bf_hi(c1)};
#pragma unroll
      for (int i = 0; i < 4; ++i) a[i] *= inv;
      LD(v2u, LB + L_ACT + (lane >> 5) * ACT_PITCH + (lane & 31) * 8) = v2u{pk_bf16(a[0], a[1]), pk_bf16(a[2], a[3])};
      if (lane == 0) LD(unsigned, LB + L_TAG) = tag;
      CBAR();
#pragma unroll
      for (int s = 0; s < 4; ++s) af[s] = LD(v4u, LB + L_ACT + (c & 1) * ACT_PITCH + (s ^ ((dph >> 1) & 3)) * 64 + g * 16);
      PIN(af[0]); PIN(af[1]); PIN(af[2]); PIN(af[3]);
    }
    STAMP2(5);
    f32x4 acc[4] = {};
#pragma unroll
    for (int s = 0; s < 4; ++s) {
      if (s == 0) asm volatile("s_waitcnt vmcnt(12)" ::: "memory");
      if (s == 1) asm volatile("s_waitcnt vmcnt(8)" ::: "memory");
      if (s == 2) asm volatile("s_waitcnt vmcnt(4)" ::: "memory");
      if (s == 3) { asm volatile("s_waitcnt vmcnt(0)" ::: "memory"); STAMP2(2); }
      v4u bf[4];
#pragma unroll
      for (int t = 0; t < 4; ++t) bf[t] = tr_frag(tra ^ (t * 32 + s * 4096), (tra ^ (t * 32 + s * 4096)) + 512);
      __builtin_amdgcn_sched_barrier(0);
#pragma unroll
      for (int t = 0; t < 4; ++t) acc[t] = mfma(af[s], bf[t], acc[t]);
      __builtin_amdgcn_sched_barrier(0);
    }
    {
      const float s0 = (g & 1) ? acc[1][0] : acc[0][0], s1 = (g & 1) ? acc[1][1] : acc[0][1];
      const float s2 = (g & 1) ? acc[3][0] : acc[2][0], s3 = (g & 1) ? acc[3][1] : acc[2][1];
      __builtin_amdgcn_raw_buffer_store_b64(v2u{(U_((g & 2) ? s2 : s0) & ~1u) | np0, tag}, rs, qoff, 0, SC1);
      __builtin_amdgcn_raw_buffer_store_b64(v2u{(U_((g & 2) ? s3 : s1) & ~1u) | np0, tag}, rs, qoff, 4096, SC1);
    }
    STAMP(3);
    __builtin_amdgcn_s_sleep(QDELAY);
    {
      v4u d[16];
#define QLOAD(d) _Pragma("unroll") for (int i = 0; i < 16; ++i) d[i] = __builtin_amdgcn_raw_buffer_load_b128(rs, goff + (i & 3) * 1024, (i >> 2) * 8192, SC1)
#define QBAD(d) ({ unsigned bad = 0; _Pragma("unroll") for (int i = 0; i < 16; ++i) bad |= (d[i].y ^ tag) | (d[i].w ^ tag); __builtin_amdgcn_ballot_w64(bad != 0u) != 0ull; })
      QLOAD(d);
      RBAR();
      STAMP2(10);
#pragma unroll 1
      for (unsigned spin = 0; spin < SPIN_CAP; ++spin) {
        if (!QBAD(d)) break;
        CBAR();
        QLOAD(d);
      }
#pragma unroll
      for (int hh = 0; hh < 4; ++hh) {
        const float lo = (F_(d[hh].x) + F_(d[4 + hh].x)) + (F_(d[8 + hh].x) + F_(d[12 + hh].x));
        const float hi = (F_(d[hh].z) + F_(d[4 + hh].z)) + (F_(d[8 + hh].z) + F_(d[12 + hh].z));
        LD(unsigned, LB + L_QS + hh * QS_PITCH + lane * 4) = pk_bf16(lo, hi);
      }
      npo = (__builtin_amdgcn_readfirstlane(d[0].x) & 1u) * 8192u;
      if (lane == 0) LD(unsigned, LB + L_TAG + 44) = npo;
    }
    BAR();
    STAMP(4);
    const u16* wsrc; unsigned wv0, wstep;
    if (j < 16) {
      wsrc = p.wo + (size_t)((kvh * 4 + (j >> 2)) * 128) * 512 + (j & 3) * 128;
      wv0 = g * 1024 + (((c >> 1) ^ g) * 32) + (c & 1) * 16; wstep = 4096;
    } else {
      wsrc = p.w1 + (kvh * 7 + (j - 16)) * 32;
      wv0 = (lane >> 2) * (FF * 2) + (((pp >> 1) ^ (lane >> 5)) * 32) + (pp & 1) * 16; wstep = 16 * FF * 2;
    }
    const unsigned wx = j < 16 ? 128u : 0u;
    const unsigned wv1 = wv0 + wstep, wv2 = (wv0 ^ wx) + 2 * wstep, wv3 = (wv0 ^ wx) + 3 * wstep;
#define W4(i_) do { const int i4 = (i_) ^ ((dph & 7) * 4); const unsigned char* s_ = (const unsigned char*)wsrc + (size_t)i4 * wstep; const unsigned l_ = LB + L_W + i4 * 1024; \
    dma16(s_, wv0, l_); dma16(s_, wv1, l_ + 1024); dma16(s_, wv2, l_ + 2048); dma16(s_, wv3, l_ + 3072); } while (0)
    const int wn1 = j < 16 ? WN1 : WN1F;
    if (j < 23) {
#pragma unroll 1
      for (int i = 0; i < wn1; i += 4) W4(i);
    }
    {
      float mw, lw;
#pragma unroll 1
      for (;;) {
        const unsigned cnt = *(volatile LAS unsigned*)(LB + L_TAG + 16);
        mw = *(volatile LAS float*)(LB + L_MX + g * 16 + pp * 4); lw = *(volatile LAS float*)(LB + L_LS + g * 16 + pp * 4);
        if (__builtin_amdgcn_readfirstlane(cnt) >= 4u) break;
        __builtin_amdgcn_s_sleep(1);
      }
      float M = vmax(mw, dpp<XOR1>(mw)); M = vmax(M, dpp<XOR2>(M));
      float S = lw * __builtin_amdgcn_exp2f(mw - M);
      S += dpp<XOR1>(S); S += dpp<XOR2>(S);
      if (c == 0)
        __builtin_amdgcn_raw_buffer_store_b128(v4u{U_(M), tag, U_(S), tag}, rs, pa_ml(kvh, g, b) + sp * 128, npo, SC1);
    }
    if (j < 23) {
      while (__builtin_amdgcn_readfirstlane(*(volatile LAS unsigned*)(LB + L_TAG + 12)) < 4u) __builtin_amdgcn_s_sleep(1);
#pragma unroll 1
      for (int i = wn1; i < 32; i += 4) W4(i);
      if (j >= 16) {
        const u16* src2 = p.w2 + (size_t)((kvh * 7 + (j - 16)) * 32) * 512;
        const unsigned u0 = lane * 16, u1 = (u0 ^ 32u) + 1024u, u2 = (u0 ^ 64u) + 2048u, u3 = (u0 ^ 96u) + 3072u;
#pragma unroll 1
        for (int i = 0; i < 8; ++i) {
          const int i4 = i ^ (dph & 7);
          const unsigned char* s_ = (const unsigned char*)src2 + i4 * 4096; const unsigned l_ = LB + L_W + 32768 + i4 * 4096, x_ = (i4 & 2) * 64;
          dma16(s_, u0 ^ x_, l_); dma16(s_, u1 ^ x_, l_ + 1024); dma16(s_, u2 ^ x_, l_ + 2048); dma16(s_, u3 ^ x_, l_ + 3072);
        }
      }
    }
  } else {
    const int w = wave - 1;
    v4u kf[8], vf[8];
#define LDV(i) LDNT(vp + (pk0 ^ (i)) * 1024 + vo)
#define DMAK(i) dma16(kp + (pk0 ^ (i)) * 1024, ko ^ (((i) & 3) * 16), LB + L_K + w * 8192 + (i) * 1024)
#pragma unroll
    for (int i = 0; i < KS1; ++i) DMAK(i);
    unsigned vo = lane * 16;
    unsigned mk0 = (c & 2) ? 0u : ((c & 1) ? 0xffff0000u : 0x0000ffffu), mk1 = (c & 2) ? ((c & 1) ? 0xffff0000u : 0x0000ffffu) : 0u;
    unsigned a_q = LB + L_QS + (c >> 2) * QS_PITCH + g * 16;
    unsigned a_mw = LB + L_MX + (c >> 2) * 16 + w * 4, a_mr = LB + L_MX + g * 16, a_mo = LB + L_MX + g * 16 + w * 4;
    unsigned a_ow = LB + L_RED + w * RED_PITCH + g * 512 + c * 16;
    unsigned a_or = LB + L_RED + w * 512 + ((lane >> 1) & 1) * 256 + (lane >> 2) * 16 + (lane & 1) * 8;
    unsigned o_pa = pa_data(kvh, w, b) + sp * 512 + lane * 8;
    unsigned a_cnt = LB + L_TAG + 4, one = 1u;
    PIN(vo); PIN(one); PIN(mk0); PIN(mk1); PIN(a_q); PIN(a_mw); PIN(a_mr); PIN(a_mo); PIN(a_ow); PIN(a_or); PIN(o_pa); PIN(a_cnt);
    unsigned a_k = LB + L_K + w * 8192 + (c & 3) * 1024 + (c >> 2) * 256 + ((g ^ c) * 16);
    PIN(a_k);
    if (KS1 < 8) {
      asm volatile("s_waitcnt vmcnt(%0)" ::"i"(KW1) : "memory");
#pragma unroll
      for (int i = KS1; i < KS2; ++i) DMAK(i);
    }
    if (KS2 < 8) {
      asm volatile("s_waitcnt vmcnt(%0)" ::"i"(KW2) : "memory");
#pragma unroll
      for (int i = KS2; i < 8; ++i) DMAK(i);
    }
    if (VW1 >= 0) asm volatile("s_waitcnt vmcnt(%0)" ::"i"(VW1 >= 0 ? VW1 : 0) : "memory");
#pragma unroll
    for (int i = 0; i < NV1; ++i) vf[i] = LDV(i);
    RBAR();
#pragma unroll
    for (int i = NV1; i < 8; ++i) vf[i] = LDV(i);
    asm volatile("s_waitcnt vmcnt(8)" ::: "memory");
#pragma unroll
    for (int i = 0; i < 8; ++i) kf[i] = LD(v4u, (a_k ^ ((i & 3) * 64)) + (i >> 2) * 4096);
#pragma unroll
    for (int i = 0; i < 8; ++i) PIN(kf[i]);
    STAMP2(1);
    __builtin_amdgcn_s_setprio(2);
    RBAR();
    STAMP(2);
    v4u qf[4];
#pragma unroll
    for (int s = 0; s < 4; ++s) qf[s] = LD(v4u, a_q + s * 64);
    tag = LD(unsigned, a_cnt - 4);
    npo = LD(unsigned, a_cnt + 40);
    __builtin_amdgcn_sched_barrier(0);
    f32x4 sc[2] = {};
#pragma unroll
    for (int s = 0; s < 4; ++s) { sc[0] = mfma(kf[s], qf[s], sc[0]); sc[1] = mfma(kf[4 + s], qf[s], sc[1]); }
    float sv[8];
#pragma unroll
    for (int i = 0; i < 4; ++i) {
      const unsigned r = pk_bf16(sc[i >> 1][(i & 1) * 2], sc[i >> 1][(i & 1) * 2 + 1]);
      const f32x2 t2 = f32x2{bf_lo(r), bf_hi(r)} * 0.08838834764831845f;
      const unsigned t = pk_bf16(t2[0], t2[1]);
      sv[2 * i] = bf_lo(t); sv[2 * i + 1] = bf_hi(t);
    }
    const float mw = xrows_max(vmax3(vmax3(sv[0], sv[1], sv[2]), vmax3(sv[3], sv[4], sv[5]), vmax(sv[6], sv[7]))) * LOG2E;
    asm volatile("s_mov_b64 exec, 0x1111\n\tds_write_b32 %0, %1\n\ts_mov_b64 exec, 1\n\tds_add_u32 %2, %3\n\ts_mov_b64 exec, -1"
                 ::"v"(a_mw), "v"(mw), "v"(a_cnt), "v"(one) : "memory");
    v2u pa[8];
    float e[8];
#pragma unroll
    for (int t = 0; t < 8; ++t) {
      e[t] = __builtin_amdgcn_exp2f(fmaf(sv[t], LOG2E, -mw));
      const unsigned pk = pk_bf16(e[t], e[t]);
      pa[t] = v2u{pk & mk0, pk & mk1};
    }
    STAMP2(3);
    o_pa += npo; PIN(o_pa);
    unsigned cnt2 = *(volatile LAS unsigned*)(a_cnt);
    f32x4 m0 = *(volatile LAS f32x4*)(a_mr);
    float mo = *(volatile LAS float*)(a_mo);
    float lw = x32_sum(x16_sum(((e[0] + e[1]) + (e[2] + e[3])) + ((e[4] + e[5]) + (e[6] + e[7]))));
    f32x4 olo = {}, ohi = {};
#pragma unroll
    for (int t = 0; t < 6; ++t) { olo = mfma16(pa[t], v2u{vf[t].x, vf[t].y}, olo); ohi = mfma16(pa[t], v2u{vf[t].z, vf[t].w}, ohi); }
    asm volatile("s_mov_b64 exec, 0x1111\n\tds_write_b32 %0, %1 offset:128\n\ts_mov_b64 exec, 1\n\tds_add_u32 %2, %3 offset:12\n\ts_mov_b64 exec, -1"
                 ::"v"(a_mw), "v"(lw), "v"(a_cnt), "v"(one) : "memory");
    __builtin_amdgcn_sched_barrier(0);
    while (__builtin_amdgcn_readfirstlane(cnt2) < 4u) {
      cnt2 = *(volatile LAS unsigned*)(a_cnt);
      m0 = *(volatile LAS f32x4*)(a_mr);
    }
    float fac = __builtin_amdgcn_exp2f(mo - vmax3(vmax(m0[0], m0[1]), m0[2], m0[3]));
    PIN(fac);
    STAMP2(10);
#pragma unroll
    for (int t = 6; t < 8; ++t) { olo = mfma16(pa[t], v2u{vf[t].x, vf[t].y}, olo); ohi = mfma16(pa[t], v2u{vf[t].z, vf[t].w}, ohi); }
    LD(f32x4, a_ow) = olo * fac;
    LD(f32x4, a_ow + 256) = ohi * fac;
    asm volatile("s_mov_b64 exec, 1\n\tds_add_u32 %0, %1 offset:4\n\ts_mov_b64 exec, -1" ::"v"(a_cnt), "v"(one) : "memory");
    STAMP2(4);
    {
      f32x2 r0, r1, r2, r3;
#pragma unroll 1
      for (;;) {
        const unsigned cnt = *(volatile LAS unsigned*)(a_cnt + 4);
        r0 = *(volatile LAS f32x2*)(a_or); r1 = *(volatile LAS f32x2*)(a_or + RED_PITCH);
        r2 = *(volatile LAS f32x2*)(a_or + 2 * RED_PITCH); r3 = *(volatile LAS f32x2*)(a_or + 3 * RED_PITCH);
        if (__builtin_amdgcn_readfirstlane(cnt) >= 4u) break;
      }
      const f32x2 a = (r0 + r1) + (r2 + r3);
      __builtin_amdgcn_raw_buffer_store_b64(v2u{pk_bf16(a[0], a[1]), tag}, rs, o_pa, 0, SC1);
    }
    asm volatile("s_mov_b64 exec, 1\n\tds_add_u32 %0, %1 offset:8\n\ts_mov_b64 exec, -1" ::"v"(a_cnt), "v"(one) : "memory");
    STAMP(5);
  }

  if (j < 16) {
    const int h = kvh * 4 + (j >> 2), cq = j & 3;
    if (wave == 0) {
      if (lane == 0) *(volatile LAS unsigned*)(LB + L_TAG + 28) = 1u;
      STAMP2(7);
      const unsigned mlo = pa_ml(kvh, j >> 2, g & 1) + c * 128 + npo;
      v4u d[1];
      asm volatile("buffer_load_dwordx4 %0, %1, %2, 0 offen sc1" : "=v"(d[0]) : "v"(mlo), "s"(rs) : "memory");
      asm volatile("s_waitcnt vmcnt(1)" ::: "memory");
      if (lane == 0) *(volatile LAS unsigned*)(LB + L_TAG + 20) = 1u;
      STAMP2(9);
      asm volatile("s_waitcnt vmcnt(0)" : "+v"(d[0])::"memory");
      if (__builtin_amdgcn_ballot_w64(((d[0].y ^ tag) | (d[0].w ^ tag)) != 0u) != 0ull) poll<1, 0>(rs, mlo, tag, d);
      const float m = F_(d[0].x);
      float M = vmax(m, dpp<XOR1>(m)); M = vmax(M, dpp<XOR2>(M)); M = vmax(M, dpp<HMIR>(M)); M = vmax(M, dpp<ROR8>(M));
      const float ex = __builtin_amdgcn_exp2f(m - M);
      const unsigned wb = pk_bf16(ex, ex);
      float S = bf_lo(wb) * F_(d[0].z);
      S += dpp<XOR1>(S); S += dpp<XOR2>(S); S += dpp<HMIR>(S); S += dpp<ROR8>(S);
      if (g < 2) *(volatile LAS unsigned*)(LB + L_WN + g * 64 + (c & 3) * 16 + (c >> 2) * 4) = wb;
      if (g < 2 && c == 0) *(volatile LAS float*)(LB + L_TAG + 32 + g * 4) = __builtin_amdgcn_rcpf(S);
      if (lane == 0) *(volatile LAS unsigned*)(LB + L_TAG + 24) = 1u;
      STAMP(6);
      STAMP_FLUSH();
      return;
    }
    const int w = wave - 1;
    const int ob = w >> 1, dh = w & 1;
    unsigned off = pa_data(kvh, j >> 2, ob) + g * 512 + dh * 256 + c * 16 + npo;
    v4u d[4];
    unsigned a_ow = LB + L_ACT + ob * ACT_PITCH + dh * 128 + c * 8;
    unsigned a_or = LB + L_ACT + (c & 1) * ACT_PITCH + g * 16;
    unsigned o_pb = g > 1 ? 0x7ffffff0u : pb_inbox(cq * 8 + 2 * w + g) + h * 128 + c * 8;
    unsigned a_wn = LB + L_WN + ob * 64 + g * 16, a_fl = LB + L_TAG + 20, a_inv = LB + L_TAG + 32 + ob * 4;
    const int q4 = (lane >> 2) & 3, pp = lane & 3;
    unsigned a_wf = LB + L_W + (8 * g + q4) * 256 + (((2 * w) ^ (q4 | ((g & 1) << 2))) * 32) + pp * 8;
    unsigned mk0 = (c & 2) ? 0u : ((c & 1) ? 0xffff0000u : 0x0000ffffu), mk1 = (c & 2) ? ((c & 1) ? 0xffff0000u : 0x0000ffffu) : 0u;
    PIN(off); PIN(a_ow); PIN(a_or); PIN(o_pb); PIN(a_wn); PIN(a_fl); PIN(a_wf); PIN(a_inv); PIN(mk0); PIN(mk1);
    while (__builtin_amdgcn_readfirstlane(*(volatile LAS unsigned*)(a_fl + 8)) == 0u) __builtin_amdgcn_s_sleep(1);
#define OLOAD() _Pragma("unroll") for (int i = 0; i < 4; ++i) d[i] = __builtin_amdgcn_raw_buffer_load_b128(rs, off + (i & 1) * 2048, (i >> 1) * 4096, SC1)
#define OOK() ({ unsigned bad = 0; _Pragma("unroll") for (int i = 0; i < 4; ++i) bad |= (d[i].y ^ tag) | (d[i].w ^ tag); __builtin_amdgcn_ballot_w64(bad != 0u) == 0ull; })
    OLOAD();
    while (__builtin_amdgcn_readfirstlane(*(volatile LAS unsigned*)(a_fl)) == 0u) {}
    CBAR();
    v4u wf[8];
#pragma unroll
    for (int s = 0; s < 8; ++s) wf[s] = tr_frag((a_wf ^ ((s & 1) * 32)) + (s >> 1) * 8192, (a_wf ^ ((s & 1) * 32)) + (s >> 1) * 8192 + 1024);
    v4u wn; float inv; unsigned fl;
#pragma unroll 1
    for (unsigned spin = 0; spin < SPIN_CAP; ++spin) {
      fl = *(volatile LAS unsigned*)(a_fl + 4);
      wn = *(volatile LAS v4u*)(a_wn); inv = *(volatile LAS float*)(a_inv);
      if (OOK()) break;
      CBAR(); OLOAD();
    }
    while (__builtin_amdgcn_readfirstlane(fl) == 0u) {
      fl = *(volatile LAS unsigned*)(a_fl + 4);
      wn = *(volatile LAS v4u*)(a_wn); inv = *(volatile LAS float*)(a_inv);
    }
    v2u wa[4];
#pragma unroll
    for (int i = 0; i < 4; ++i) wa[i] = v2u{wn[i] & mk0, wn[i] & mk1};
    STAMP(6);
    {
      f32x4 o = {};
#pragma unroll
      for (int i = 0; i < 4; ++i) o = mfma16(wa[i], v2u{d[i].x, d[i].z}, o);
      o *= inv;
      if (g == 0) LD(v2u, a_ow) = v2u{pk_bf16(o[0], o[1]), pk_bf16(o[2], o[3])};
    }
    BAR();
    f32x4 acc0 = {}, acc1 = {};
    v4u of[4];
#pragma unroll
    for (int s = 0; s < 4; ++s) of[s] = LD(v4u, a_or + s * 64);
    __builtin_amdgcn_sched_barrier(0);
#pragma unroll
    for (int s = 0; s < 4; ++s) { acc0 = mfma(of[s], wf[2 * s], acc0); acc1 = mfma(of[s], wf[2 * s + 1], acc1); }
    __builtin_amdgcn_raw_buffer_store_b64(v2u{g ? pk_bf16(acc1[0], acc1[1]) : pk_bf16(acc0[0], acc0[1]), tag}, rs, o_pb, 0, SC1);
    STAMP(7);
  } else if (j < 23) {
    const int f = kvh * 7 + (j - 16);
    if (wave == 0) {
      asm volatile("s_waitcnt vmcnt(0)" ::: "memory");
      STAMP(6);
      RBAR();
      RBAR();
      unsigned a_h = LB + L_H + ((lane >> 2) & 1) * H_PITCH + (lane >> 3) * 32 + (lane & 3) * 8;
      unsigned a_st = LB + L_WN + ((lane >> 2) & 1) * 8;
      PIN(a_h); PIN(a_st);
      v4u d[4];
      poll<4, 1024>(rs, WS_HG + (kvh & 1) * 16384 + npo + ((kvh >> 1) & 1) * 4096 + lane * 16, tag, d);
      STAMP(7);
#pragma unroll
      for (int i = 0; i < 4; ++i) LD(v2u, a_h + i * 256) = v2u{d[i].x, d[i].z};
      BAR();
      float s0 = 0.f, s1 = 0.f, q0 = 0.f, q1 = 0.f;
#pragma unroll
      for (int i = 0; i < 4; ++i) {
        s0 = dot2(d[i].x, 0x3f803f80u, s0); s1 = dot2(d[i].z, 0x3f803f80u, s1);
        q0 = dot2(d[i].x, d[i].x, q0); q1 = dot2(d[i].z, d[i].z, q1);
      }
      float t = x16_sum2(s0 + s1, q0 + q1);
      t += dpp<XOR1>(t); t += dpp<XOR2>(t); t += dpp<ROR8>(t);
      t = x32_sum(t) * (1.0f / 512.0f);
      auto u = __builtin_amdgcn_permlane16_swap(U_(t), U_(t), false, false);
      const float mean = F_(u[0]);
      const float var = fmaxf(fmaf(-mean, mean, F_(u[1])), 0.f);
      const float inv = __builtin_amdgcn_rcpf(rbf(__builtin_amdgcn_sqrtf(rbf(rbf(var) + 1e-5f))));
      LD(f32x2, a_st) = f32x2{rbf(mean), inv};
      BAR();
      STAMP(8);
      STAMP_FLUSH();
      return;
    }
    const int w = wave - 1;
    float bias1 = bf_lo(p.b1[f * 32 + (lane & 31)]);
    RBAR();
    v4u w1f[8], w2f[8];
    {
      const int q4 = (lane >> 2) & 3, pp = lane & 3;
      const unsigned b1a = LB + L_W + (128 * w + 8 * g + q4) * 64 + ((g & 1) * 32) + pp * 8;
#pragma unroll
      for (int s = 0; s < 8; ++s) w1f[s] = tr_frag((b1a ^ ((s & 1) * 32)) + (s >> 1) * 2048, (b1a ^ ((s & 1) * 32)) + (s >> 1) * 2048 + 256);
      const unsigned b2a = LB + L_W + 32768 + (8 * g + q4) * 1024 + w * 256 + ((q4 | ((g & 1) << 2)) * 32) + pp * 8;
#pragma unroll
      for (int t = 0; t < 8; ++t) w2f[t] = tr_frag(b2a ^ (t * 32), (b2a ^ (t * 32)) + 4096);
    }
    const f32x4 z4 = {};
    unsigned a_zw = LB + L_ZP + ((g & 1) * 32 + (g >> 1) * 16 + c) * 16 + w * 4;
    unsigned a_zr = LB + L_ZP + lane * 16;
    unsigned a_ha = LB + L_H + (c & 1) * H_PITCH + w * 256 + g * 16;
    unsigned a_st = LB + L_WN + (lane >> 5) * 8;
    unsigned a_gw = LB + L_GA + w * 160 + (lane >> 5) * 80 + (lane & 31) * 2, a_gr = LB + L_GA + w * 160 + (c & 1) * 80 + g * 16;
    unsigned o_pd = WS_PD + (8 * w + g) * 8192 + f * 128 + c * 8;
    float csum;
    {
      const v4u ones = {0x3f803f80u, 0x3f803f80u, 0x3f803f80u, 0x3f803f80u};
      f32x4 c0 = z4, c1 = z4;
#pragma unroll
      for (int s = 0; s < 4; ++s) { c0 = mfma(ones, w1f[2 * s], c0); c1 = mfma(ones, w1f[2 * s + 1], c1); }
      LD(float, a_zw) = (g & 2) ? c1[0] : c0[0];
      BAR();
      const f32x4 a = LD(f32x4, a_zr);
      csum = (a[0] + a[1]) + (a[2] + a[3]);
      asm volatile("" : "+v"(csum), "+v"(w2f[0]), "+v"(w2f[1]), "+v"(w2f[2]), "+v"(w2f[3]), "+v"(w2f[4]), "+v"(w2f[5]), "+v"(w2f[6]), "+v"(w2f[7]), "+v"(bias1));
      PIN(a_zw); PIN(a_zr); PIN(a_ha); PIN(a_st); PIN(a_gw); PIN(a_gr); PIN(o_pd);
    }
    BAR();
    STAMP2(7);
    {
      v4u ha[4];
#pragma unroll
      for (int s = 0; s < 4; ++s) ha[s] = LD(v4u, a_ha + s * 64);
      __builtin_amdgcn_sched_barrier(0);
      f32x4 z0 = z4, z1 = z4;
#pragma unroll
      for (int s = 0; s < 4; ++s) { z0 = mfma(ha[s], w1f[2 * s], z0); z1 = mfma(ha[s], w1f[2 * s + 1], z1); }
      const float t0 = (g & 1) ? z0[1] : z0[0], t1 = (g & 1) ? z1[1] : z1[0];
      LD(float, a_zw) = (g & 2) ? t1 : t0;
    }
    BAR();
    {
      const f32x4 a = LD(f32x4, a_zr);
      const f32x2 st = LD(f32x2, a_st);
      const float zr = (a[0] + a[1]) + (a[2] + a[3]);
      const float z = rbf(rbf(fmaf(-st[0], csum, zr) * st[1]) + bias1);
      const float t = fmaf(z * z, 0.044715f * 0.7978845608028654f * 2.f * LOG2E, 0.7978845608028654f * 2.f * LOG2E);
      const float e = __builtin_amdgcn_exp2f(z * t);
      const float gl = fmaf(-z, __builtin_amdgcn_rcpf(e + 1.0f), z);
      LD(u16, a_gw) = (u16)pk_bf16(gl, gl);
    }
    CBAR();
    const v4u ga = LD(v4u, a_gr);
#pragma unroll
    for (int u = 0; u < 2; ++u) {
      const f32x4 y0 = mfma(ga, w2f[4 * u], z4), y1 = mfma(ga, w2f[4 * u + 1], z4), y2 = mfma(ga, w2f[4 * u + 2], z4), y3 = mfma(ga, w2f[4 * u + 3], z4);
      const unsigned p0 = pk_bf16(y0[0], y0[1]), p1 = pk_bf16(y1[0], y1[1]), p2 = pk_bf16(y2[0], y2[1]), p3 = pk_bf16(y3[0], y3[1]);
      __builtin_amdgcn_raw_buffer_store_b64(v2u{(g & 2) ? ((g & 1) ? p3 : p2) : ((g & 1) ? p1 : p0), tag}, rs, o_pd, u * 4 * 8192, SC1);
    }
    STAMP(8);
  } else if (j < 31 && wave == 0) {
    const unsigned np = npo >> 13;
    const unsigned role = claim_role(p.ws, (((j - 23) * 4 + (kvh & 3)) * 2) | np, tag);
    const int r = role & 31, n = lane & 7, pg = lane >> 3;
    if (j < 27) {
      unsigned xr = *(const unsigned*)(p.x + (g & 1) * 512 + r * 16 + 2 * n);
      unsigned o_hg = WS_HG + ((lane >> 3) & 1) * 8192 + (g >> 1) * 4096 + r * 128 + ((g & 1) * 8 + n) * 8;
      PIN(xr); PIN(o_hg);
      STAMP2(9);
      v4u d[4];
      poll<4, 1024>(rs, pb_inbox(r) + lane * 16, tag, d);
      STAMP(6);
      const float a0 = (bf_lo(d[0].x) + bf_lo(d[1].x)) + (bf_lo(d[2].x) + bf_lo(d[3].x)), a1 = (bf_hi(d[0].x) + bf_hi(d[1].x)) + (bf_hi(d[2].x) + bf_hi(d[3].x));
      const float b0 = (bf_lo(d[0].z) + bf_lo(d[1].z)) + (bf_lo(d[2].z) + bf_lo(d[3].z)), b1 = (bf_hi(d[0].z) + bf_hi(d[1].z)) + (bf_hi(d[2].z) + bf_hi(d[3].z));
      float t = x32_sum2(x16_sum2(a0, a1), x16_sum2(b0, b1));
      t += dpp<ROR8>(t);
      auto sw = __builtin_amdgcn_permlane32_swap(U_(t), U_(t), false, false);
      const unsigned ar = pk_bf16(F_(sw[0]), F_(sw[1]));
      const unsigned pk = pk_bf16(bf_lo(xr) + bf_lo(ar), bf_hi(xr) + bf_hi(ar));
      __builtin_amdgcn_raw_buffer_store_b64(v2u{pk, tag}, rs, o_hg, 0, SC1);
      __builtin_amdgcn_raw_buffer_store_b64(v2u{pk, tag}, rs, o_hg, 16384, SC1);
      __builtin_amdgcn_raw_buffer_store_b64(v2u{pk, tag}, rs, o_hg, 32768, SC1);
      if (r == 0 && lane == 0) __builtin_amdgcn_raw_buffer_store_b32(tag, rs, WS_EPOCH, 0, SC1);
      STAMP(7);
    } else {
      unsigned b2r = *(const unsigned*)(p.b2 + r * 16 + 2 * n);
      v2u hg;
      const unsigned hoff = WS_HG + 32768 + np * 8192 + r * 128 + ((g & 1) * 8 + n) * 8;
#pragma unroll 1
      for (unsigned spin = 0; spin < SPIN_CAP; ++spin) {
        CBAR();
        hg = __builtin_amdgcn_raw_buffer_load_b64(rs, hoff, 0, SC1);
        if (__all(hg.y == tag)) break;
        __builtin_amdgcn_s_sleep(2);
      }
      unsigned o_out = ((g & 1) * 512 + r * 16 + 2 * n) * 2;
      PIN(b2r); PIN(o_out);
      if (RDDELAY) __builtin_amdgcn_s_sleep(RDDELAY);
      v4u d[7];
      poll<7, 1024>(rs, WS_PD + r * 8192 + lane * 16, tag, d);
      STAMP(8);
#define S7(f, m) (((f(d[0].m) + f(d[1].m)) + (f(d[2].m) + f(d[3].m))) + ((f(d[4].m) + f(d[5].m)) + f(d[6].m)))
      float t = x32_sum2(x16_sum2(S7(bf_lo, x), S7(bf_hi, x)), x16_sum2(S7(bf_lo, z), S7(bf_hi, z)));
      t += dpp<ROR8>(t);
      auto sw = __builtin_amdgcn_permlane32_swap(U_(t), U_(t), false, false);
      const unsigned ar = pk_bf16(F_(sw[0]), F_(sw[1]));
      const unsigned yr = pk_bf16(bf_lo(ar) + bf_lo(b2r), bf_hi(ar) + bf_hi(b2r));
      const unsigned pk = pk_bf16(bf_lo(hg.x) + bf_lo(yr), bf_hi(hg.x) + bf_hi(yr));
      if ((lane & 40) == 0) *(unsigned*)((unsigned char*)p.out + o_out) = pk;
      STAMP(9);
    }
  }
  STAMP_FLUSH();
}

}

extern "C" void kernel_launch(void* const* d_in, const int* in_sizes, int n_in,
                              void* d_out, int out_size, void* d_ws, size_t ws_size,
                              hipStream_t stream) {
  Params p;
  p.x = (const u16*)d_in[0]; p.kc = (const u16*)d_in[1]; p.vc = (const u16*)d_in[2];
  p.wq = (const u16*)d_in[3]; p.wo = (const u16*)d_in[4];
  p.w1 = (const u16*)d_in[6]; p.b1 = (const u16*)d_in[7]; p.w2 = (const u16*)d_in[8]; p.b2 = (const u16*)d_in[9];
  p.out = (u16*)d_out; p.ws = (unsigned char*)(((uintptr_t)d_ws + 16383) & ~(uintptr_t)16383);
  (void)hipFuncSetAttribute((const void*)fused_decode, hipFuncAttributeMaxDynamicSharedMemorySize, LDS_BYTES);
  hipLaunchKernelGGL(fused_decode, dim3(256), dim3(NTHR), LDS_BYTES, stream, p);
}
```

```cpp
#include <hip/hip_runtime.h>
#include <hip/hip_bf16.h>
#include <stdint.h>

typedef unsigned short u16;
typedef unsigned long long u64;
typedef unsigned v4u __attribute__((ext_vector_type(4)));
typedef unsigned v2u __attribute__((ext_vector_type(2)));
typedef float f32x4 __attribute__((ext_vector_type(4)));
typedef float f32x2 __attribute__((ext_vector_type(2)));
typedef short bf16x8 __attribute__((ext_vector_type(8)));
typedef short bf16x4 __attribute__((ext_vector_type(4)));
typedef __bf16 bf16x2 __attribute__((ext_vector_type(2)));
typedef short v4i16 __attribute__((ext_vector_type(4)));
#define LAS __attribute__((address_space(3)))
#define GAS __attribute__((address_space(1)))

namespace {

constexpr int SK = 8192, WIN = 2048, FF = 1792;
constexpr int NTHR = 320;
#define QDELAY 24
#define PACE 1
#define WN1 32
#ifndef NV0
#define NV0 0
#endif
#define QDELAY2 0
#define WN1F 16
#ifndef NV1
#define NV1 2
#endif
#define KS1 4
#define KW1 3
#define KS2 8
#define KW2 0
#define RDDELAY 0
#define HOMING 1
#define NEARP(ws) near_parity(ws)
#define FBOTH 0
#define VW1 5
#define DEPH 1
#define BK 1
#ifndef NV2
#define NV2 4
#define VW2 5
#define VW3 5
#endif
#define PFNT ""
#define QHOME 1
#define B0POS 1
#define LOOK2 0
#define WEARLY 0
#define WN1E 32
#define WN1FE 16
#define FDELAY 0
#define CODE_KB 14
constexpr unsigned SPIN_CAP = 1u << 18;
constexpr float LOG2E = 1.4426950408889634f;

constexpr unsigned WS_EPOCH = 0;
constexpr unsigned WS_CLAIM = 1024;
constexpr unsigned WS_CAL = 16384;
constexpr unsigned WS_HG = WS_CAL + 65536;
constexpr unsigned WS_PB = WS_HG + 3 * 16384;
constexpr unsigned WS_PD = WS_PB + 8 * 16384;
constexpr unsigned WS_QG = WS_PD + 32 * 8192;
constexpr unsigned WS_PA = WS_QG + 8 * 4 * 16384;
constexpr unsigned WS_END = WS_PA + 8 * 9 * 16384;
__host__ __device__ constexpr unsigned pa_data(unsigned kvh, unsigned hh, unsigned b) { return WS_PA + (kvh * 9 + hh * 2 + b) * 16384; }
__host__ __device__ constexpr unsigned pa_ml(unsigned kvh, unsigned hh, unsigned b) { return WS_PA + (kvh * 9 + 8) * 16384 + b * 4096 + hh * 8; }
constexpr unsigned WS_STAMP = 4u << 20;
__host__ __device__ constexpr unsigned pb_inbox(unsigned r) { return WS_PB + (r >> 2) * 16384 + (r & 1) * 8192 + ((r >> 1) & 1) * 4096; }

constexpr int L_WQ = 0;
constexpr int RED_PITCH = 2048;
constexpr int L_RED = 0;
constexpr int L_W = 17408;
constexpr int L_K = L_W + 32768;
constexpr int L_ACT = L_W + 65536;
constexpr int ACT_PITCH = 256 + 64;
constexpr int L_QS = L_ACT + 2 * ACT_PITCH;
constexpr int QS_PITCH = 256 + 64;
constexpr int L_MX = L_QS + 4 * QS_PITCH;
constexpr int L_LS = L_MX + 128;
constexpr int L_WN = L_LS + 128;
constexpr int L_ZP = L_WN + 128;
constexpr int L_GA = L_ZP + 64 * 48;
constexpr int L_H = L_GA + 8 * 160;
constexpr int H_PITCH = 1024 + 64;
constexpr int L_TAG = L_H + 2 * H_PITCH;
constexpr int L_PF = L_TAG + 64;
constexpr int LDS_BYTES = L_PF + 1024;
static_assert(8 * RED_PITCH <= L_W, "lds map");
static_assert(LDS_BYTES > 81920, "one workgroup per CU");

struct Params {
  const u16* x; const u16* kc; const u16* vc; const u16* wq; const u16* wo;
  const u16* w1; const u16* b1; const u16* w2; const u16* b2;
  u16* out; unsigned char* ws;
};

#define F_(v) __uint_as_float(v)
#define U_(v) __float_as_uint(v)
__device__ __forceinline__ float bf_lo(unsigned d) { return F_(d << 16); }
__device__ __forceinline__ float bf_hi(unsigned d) { return F_(d & 0xffff0000u); }
__device__ __forceinline__ unsigned pk_bf16(float lo, float hi) {
  return __builtin_bit_cast(unsigned, __builtin_convertvector(f32x2{lo, hi}, bf16x2));
}
__device__ __forceinline__ float rbf(float f) { return bf_lo(pk_bf16(f, f)); }
__device__ __forceinline__ f32x4 mfma(v4u a, v4u b, f32x4 c) {
  return __builtin_amdgcn_mfma_f32_16x16x32_bf16(__builtin_bit_cast(bf16x8, a), __builtin_bit_cast(bf16x8, b), c, 0, 0, 0);
}
__device__ __forceinline__ f32x4 mfma16(v2u a, v2u b, f32x4 c) {
  return __builtin_amdgcn_mfma_f32_16x16x16bf16_1k(__builtin_bit_cast(bf16x4, a), __builtin_bit_cast(bf16x4, b), c, 0, 0, 0);
}
__device__ __forceinline__ float dot2(unsigned a, unsigned b, float acc) {
  return __builtin_amdgcn_fdot2_f32_bf16(__builtin_bit_cast(bf16x2, a), __builtin_bit_cast(bf16x2, b), acc, false);
}
template <int CTRL> __device__ __forceinline__ float dpp(float x) {
  return __builtin_bit_cast(float, __builtin_amdgcn_mov_dpp(__builtin_bit_cast(int, x), CTRL, 0xf, 0xf, true));
}
constexpr int XOR1 = 0xB1, XOR2 = 0x4E, HMIR = 0x141, ROR8 = 0x128;
__device__ __forceinline__ float x16_sum(float x) {
  auto s = __builtin_amdgcn_permlane16_swap(U_(x), U_(x), false, false);
  return F_(s[0]) + F_(s[1]);
}
__device__ __forceinline__ float x32_sum(float x) {
  auto s = __builtin_amdgcn_permlane32_swap(U_(x), U_(x), false, false);
  return F_(s[0]) + F_(s[1]);
}
__device__ __forceinline__ float x16_sum2(float a, float b) {
  auto s = __builtin_amdgcn_permlane16_swap(U_(a), U_(b), false, false);
  return F_(s[0]) + F_(s[1]);
}
__device__ __forceinline__ float x32_sum2(float a, float b) {
  auto s = __builtin_amdgcn_permlane32_swap(U_(a), U_(b), false, false);
  return F_(s[0]) + F_(s[1]);
}
__device__ __forceinline__ float vmax(float a, float b) { float r; asm("v_max_f32 %0, %1, %2" : "=v"(r) : "v"(a), "v"(b)); return r; }
__device__ __forceinline__ float vmax3(float a, float b, float c) { float r; asm("v_max3_f32 %0, %1, %2, %3" : "=v"(r) : "v"(a), "v"(b), "v"(c)); return r; }
__device__ __forceinline__ float xrows_max(float x) {
  auto s = __builtin_amdgcn_permlane16_swap(U_(x), U_(x), false, false);
  x = vmax(F_(s[0]), F_(s[1]));
  auto t = __builtin_amdgcn_permlane32_swap(U_(x), U_(x), false, false);
  return vmax(F_(t[0]), F_(t[1]));
}
#define BAR() asm volatile("s_waitcnt lgkmcnt(0)\n\ts_barrier" ::: "memory")
#define RBAR() asm volatile("s_barrier" ::: "memory")
#define CBAR() asm volatile("" ::: "memory")
#define PIN(x) asm volatile("" : "+v"(x))
#define LDNT(p) __builtin_nontemporal_load((const v4u*)(p))
#define SC1 16
#define LD(T, a) (*(LAS T*)(a))

__device__ __forceinline__ void dma16(const void* sbase, unsigned voff, unsigned ldsaddr) {
  asm volatile("s_mov_b32 m0, %2\n\ts_nop 0\n\tglobal_load_lds_dwordx4 %0, %1 nt" ::"v"(voff), "s"(sbase), "s"(ldsaddr) : "memory");
}
__device__ __forceinline__ v4u tr_frag(unsigned a0, unsigned a1) {
  v2u a = __builtin_bit_cast(v2u, __builtin_amdgcn_ds_read_tr16_b64_v4i16((LAS v4i16*)a0));
  v2u b = __builtin_bit_cast(v2u, __builtin_amdgcn_ds_read_tr16_b64_v4i16((LAS v4i16*)a1));
  return v4u{a.x, a.y, b.x, b.y};
}
__device__ __forceinline__ v4u ldg(__amdgpu_buffer_rsrc_t rs, unsigned off) { return __builtin_amdgcn_raw_buffer_load_b128(rs, off, 0, SC1); }
__device__ __forceinline__ void lds_arrive(unsigned addr) {
  asm volatile("s_mov_b64 exec, 1\n\tds_add_u32 %0, %1\n\ts_mov_b64 exec, -1" ::"v"(addr), "v"(1u) : "memory");
}

__device__ __forceinline__ void cal_issue(const unsigned char* ws) {
  const unsigned o = WS_CAL + (blockIdx.x >> 6) * 16384 + (blockIdx.x & 63) * 128;
  asm volatile("s_mov_b64 exec, 1\n\tglobal_store_dword %0, %0, %2 sc1\n\tglobal_store_dword %1, %0, %2 sc1\n\ts_mov_b64 exec, -1"
               ::"v"(o), "v"(o + 8192), "s"(ws) : "memory");
}
__device__ __forceinline__ unsigned cal_result() {
  u64 t1, t2;
  asm volatile("s_waitcnt vmcnt(1)\n\ts_memtime %0\n\ts_waitcnt vmcnt(0)\n\ts_memtime %1\n\ts_waitcnt lgkmcnt(0)" : "=&s"(t1), "=&s"(t2)::"memory");
  return (((unsigned)t2 - (unsigned)t1) > 100u ? 0u : 1u) ^ (HOMING == 2 ? 1u : 0u);
}
__device__ __forceinline__ unsigned claim_role(unsigned char* ws, unsigned role, unsigned tag) {
  unsigned old = 0;
  if ((threadIdx.x & 63) == 0) old = __hip_atomic_exchange((GAS unsigned*)(ws + WS_CLAIM + role * 128), tag, __ATOMIC_RELAXED, __HIP_MEMORY_SCOPE_AGENT);
  return __builtin_amdgcn_readfirstlane(old) == tag ? role ^ 1u : role;
}

__device__ __forceinline__ unsigned tagbad(unsigned bad, v4u d, unsigned tag) {
  return __builtin_amdgcn_bitop3_b32(__builtin_amdgcn_bitop3_b32(bad, d.y, tag, 0xF6), d.w, tag, 0xF6);
}
#define ANY(bad) (__builtin_amdgcn_ballot_w64((bad) != 0u) != 0ull)
template <int N, int STRIDE>
__device__ __forceinline__ void poll(__amdgpu_buffer_rsrc_t rs, unsigned off, unsigned tag, v4u (&d)[N]) {
  unsigned n = SPIN_CAP;
#pragma unroll 1
  for (;;) {
    asm volatile("" : "+s"(n)::"memory");
#pragma unroll
    for (int i = 0; i < N; ++i) d[i] = __builtin_amdgcn_raw_buffer_load_b128(rs, off + (i & 3) * STRIDE, (i >> 2) * 4 * STRIDE, SC1);
    unsigned bad = 0;
#pragma unroll
    for (int i = 0; i < N; ++i) bad = tagbad(bad, d[i], tag);
    if (!ANY(bad)) break;
    if (--n == 0u) break;
  }
}

__device__ __forceinline__ float half_sum(float x) {
  x += dpp<XOR1>(x); x += dpp<XOR2>(x); x += dpp<HMIR>(x); x += dpp<ROR8>(x);
  return x16_sum(x);
}
__device__ __forceinline__ void ln_stats(v4u a, v4u b, float& mb, float& inv) {
  float s0 = 0.f, s1 = 0.f;
#pragma unroll
  for (int i = 0; i < 4; ++i) { s0 = dot2(a[i], 0x3f803f80u, s0); s1 = dot2(b[i], 0x3f803f80u, s1); }
  const float mean = half_sum(s0 + s1) * (1.0f / 512.0f);
  f32x2 q0 = {0.f, 0.f}, q1 = {0.f, 0.f};
#pragma unroll
  for (int i = 0; i < 4; ++i) {
    const f32x2 u = f32x2{bf_lo(a[i]), bf_hi(a[i])} - mean, v = f32x2{bf_lo(b[i]), bf_hi(b[i])} - mean;
    q0 = u * u + q0; q1 = v * v + q1;
  }
  q0 += q1;
  const float var = half_sum(q0[0] + q0[1]) * (1.0f / 512.0f);
  mb = rbf(mean);
  inv = __builtin_amdgcn_rcpf(rbf(__builtin_amdgcn_sqrtf(rbf(rbf(var) + 1e-5f))));
}

#define STAMP(i) do {} while (0)
#define STAMP_DECL
#define STAMP_FLUSH() do {} while (0)
#define STAMP2(i) do {} while (0)

__global__ void __launch_bounds__(NTHR, 1) fused_decode(Params p) {
  extern __shared__ __attribute__((aligned(16))) unsigned char lds_raw[];
  u64 pc;
  asm volatile("s_getpc_b64 %0" : "=s"(pc));
  const unsigned tstart = (unsigned)__builtin_amdgcn_s_memrealtime();
  const unsigned LB = (unsigned)(size_t)(LAS unsigned char*)lds_raw;
  const int tid = threadIdx.x, lane = tid & 63, hwave = __builtin_amdgcn_readfirstlane(tid >> 6);
  const int wave = hwave == 4 ? 0 : hwave + 1;
  const int kvh = blockIdx.x & 7, j = blockIdx.x >> 3;
  const int g = lane >> 4, c = lane & 15;
  STAMP_DECL;
  STAMP(0);
  const __amdgpu_buffer_rsrc_t rs = __builtin_amdgcn_make_buffer_rsrc((void*)p.ws, 0, WS_END, 0x00020000);
  const int b = j >> 4, sp = j & 15;
  unsigned tag, npo;

  const int rg = j >> 3, cg = j & 7;
  const int dph = DEPH ? (j + kvh * 5) : 0;
  unsigned ep; v4u x0, x1; v2u xs;
  asm volatile("" ::"s"(p.ws), "s"(p.kc), "s"(p.out));
  unsigned pf0 = 0, pf1 = 0, pf2 = 0;
  if (wave > 1) {
    unsigned o = (j * 3 + wave - 2) * 192;
    o = o < CODE_KB * 1024 - 192 ? o : CODE_KB * 1024 - 192;
    asm volatile("s_load_dword %0, %3, 0x0\n\ts_load_dword %1, %3, 0x40\n\ts_load_dword %2, %3, 0x80" : "=&s"(pf0), "=&s"(pf1), "=&s"(pf2) : "s"((pc & ~255ull) + o), "s"(p.ws), "s"(p.kc), "s"(p.out));
  }
  if (wave == 1) { cal_issue(p.ws); const unsigned np0 = cal_result(); if (lane == 0) LD(unsigned, LB + L_TAG + 40) = np0; asm volatile("s_waitcnt lgkmcnt(0)" ::: "memory"); }
  if (wave == 0) {
    __builtin_amdgcn_s_setprio(3);
    asm volatile("global_load_dword %0, %1, %2 sc1" : "=v"(ep) : "v"(WS_EPOCH), "s"(p.ws) : "memory");
    asm volatile("global_load_dwordx4 %0, %1, %2" : "=v"(x0) : "v"(lane * 32), "s"(p.x) : "memory");
    asm volatile("global_load_dwordx4 %0, %1, %2 offset:16" : "=v"(x1) : "v"(lane * 32), "s"(p.x) : "memory");
    asm volatile("global_load_dwordx2 %0, %1, %2" : "=v"(xs) : "v"((lane >> 5) * 1024 + rg * 256 + (lane & 31) * 8), "s"(p.x) : "memory");
    const int r0 = lane >> 3, pos = lane & 7;
    const unsigned v0 = r0 * 8192 + (((pos >> 1) ^ ((r0 >> 1) & 1)) * 32) + (pos & 1) * 16;
    const u16* src = p.wq + (size_t)(rg * 128) * 4096 + kvh * 512 + cg * 64;
    const unsigned v1 = (v0 ^ 64u) + 65536u, va = (dph & 8) ? v1 : v0, vb = (dph & 8) ? v0 : v1, la = (dph & 8) ? 1024u : 0u;
#pragma unroll 1
    for (int i = 0; i < 8; ++i) {
      const int ii = i ^ (dph & 7);
      dma16(src + (size_t)ii * 16 * 4096, va, LB + L_WQ + ii * 2048 + la);
      dma16(src + (size_t)ii * 16 * 4096, vb, LB + L_WQ + ii * 2048 + (la ^ 1024));
    }
  }
  const size_t kvbase = ((size_t)(b * 8 + kvh) * SK + (SK - WIN) + sp * 128) * 128;
  const unsigned char* kp = (const unsigned char*)(p.kc + kvbase);
  const unsigned char* vp = (const unsigned char*)(p.vc + kvbase);
  const int pk0 = (((wave - 1) * 8) ^ dph) & 31;
  unsigned ko = g * 256 + ((c ^ (4 * g)) * 16);
  PIN(ko);
  if (B0POS == 0) RBAR();
  {
    int wave1 = wave;
    asm volatile("" : "+s"(wave1));
    if (wave1 != 0) {
      if (B0POS) RBAR();
      const int w = wave - 1;
#define DMAK(i) dma16(kp + (pk0 ^ (i)) * 1024, ko ^ (((i) & 3) * 16), LB + L_K + w * 8192 + (i) * 1024)
#pragma unroll
      for (int i = 0; i < KS1; ++i) DMAK(i);
    }
  }
  int wave2 = wave;
  asm volatile("" : "+s"(wave2));

  if (__builtin_expect(wave2 == 0, 1)) {
    if (lane < 8) LD(unsigned, LB + L_TAG + 4 + lane * 4) = 0u;
    const int q4 = (lane >> 2) & 3, pp = lane & 3;
    unsigned tra = LB + L_WQ + (8 * g + q4) * 128 + pp * 8 + ((((q4 >> 1) & 1) | ((g & 1) << 1)) * 32) + ((dph >> 1) & 3) * 4096;
    unsigned qoff = WS_QG + (kvh * 4 + rg) * 16384 + (cg * 64 + lane) * 8;
    unsigned a_ln = LB + L_ACT + (lane >> 5) * ACT_PITCH + (lane & 31) * 8;
    unsigned a_af = LB + L_ACT + (c & 1) * ACT_PITCH + ((dph >> 1) & 3) * 64;
    unsigned af0 = a_af + g * 16, af1 = (((dph >> 1) & 3) ^ 1) * 64 + LB + L_ACT + (c & 1) * ACT_PITCH + g * 16;
    unsigned af2 = (((dph >> 1) & 3) ^ 2) * 64 + LB + L_ACT + (c & 1) * ACT_PITCH + g * 16, af3 = (((dph >> 1) & 3) ^ 3) * 64 + LB + L_ACT + (c & 1) * ACT_PITCH + g * 16;
    unsigned trs[16];
#pragma unroll
    for (int i = 0; i < 16; ++i) { trs[i] = tra ^ ((i & 3) * 32 + (i >> 2) * 4096); PIN(trs[i]); }
    PIN(qoff); PIN(a_ln); PIN(af0); PIN(af1); PIN(af2); PIN(af3);
    asm volatile("s_waitcnt vmcnt(16)" : "+v"(ep), "+v"(x0), "+v"(x1), "+v"(xs)::"memory");
    STAMP2(1);
    if (B0POS == 1) RBAR();
    v4u af[4];
    {
      float mb, inv;
      ln_stats(x0, x1, mb, inv);
      const unsigned c0 = pk_bf16(bf_lo(xs.x) - mb, bf_hi(xs.x) - mb), c1 = pk_bf16(bf_lo(xs.y) - mb, bf_hi(xs.y) - mb);
      LD(v2u, a_ln) = v2u{pk_bf16(bf_lo(c0) * inv, bf_hi(c0) * inv), pk_bf16(bf_lo(c1) * inv, bf_hi(c1) * inv)};
      CBAR();
      af[0] = LD(v4u, af0); af[1] = LD(v4u, af1); af[2] = LD(v4u, af2); af[3] = LD(v4u, af3);
      CBAR();
    }
    if (B0POS == 2) RBAR();
    tag = __builtin_amdgcn_readfirstlane(ep) + 1u;
    if (lane == 0) LD(unsigned, LB + L_TAG) = tag;
    unsigned np0;
    if (B0POS < 3) np0 = LD(unsigned, LB + L_TAG + 40);
    STAMP2(5);
    f32x4 acc[4] = {};
#pragma unroll
    for (int s = 0; s < 4; ++s) {
      if (s == 0) asm volatile("s_waitcnt vmcnt(12)" ::: "memory");
      if (s == 1) asm volatile("s_waitcnt vmcnt(8)" ::: "memory");
      if (s == 2) { if (B0POS == 3) { RBAR(); np0 = LD(unsigned, LB + L_TAG + 40); } asm volatile("s_waitcnt vmcnt(4)" ::: "memory"); }
      if (s == 3) { asm volatile("s_waitcnt vmcnt(0)" ::: "memory"); STAMP2(2); }
      v4u bf[4];
#pragma unroll
      for (int t = 0; t < 4; ++t) bf[t] = tr_frag(trs[4 * s + t], trs[4 * s + t] + 512);
      __builtin_amdgcn_sched_barrier(0);
#pragma unroll
      for (int t = 0; t < 4; ++t) acc[t] = mfma(af[s], bf[t], acc[t]);
      __builtin_amdgcn_sched_barrier(0);
    }
    {
      const float s0 = (g & 1) ? acc[1][0] : acc[0][0], s1 = (g & 1) ? acc[1][1] : acc[0][1];
      const float s2 = (g & 1) ? acc[3][0] : acc[2][0], s3 = (g & 1) ? acc[3][1] : acc[2][1];
      const v2u q0 = {(U_((g & 2) ? s2 : s0) & ~1u) | np0, tag}, q1 = {(U_((g & 2) ? s3 : s1) & ~1u) | np0, tag};
      const unsigned n0 = QHOME ? __builtin_amdgcn_readfirstlane(np0) * 8192u : 0u;
      __builtin_amdgcn_raw_buffer_store_b64(q0, rs, qoff, n0, SC1);
      __builtin_amdgcn_raw_buffer_store_b64(q1, rs, qoff, n0 + 4096, SC1);
      if (QHOME) {
        __builtin_amdgcn_raw_buffer_store_b64(q0, rs, qoff, n0 ^ 8192u, SC1);
        __builtin_amdgcn_raw_buffer_store_b64(q1, rs, qoff, (n0 ^ 8192u) + 4096, SC1);
      }
    }
    if (j == 0 && lane == 0) __builtin_amdgcn_raw_buffer_store_b64(v2u{tstart, tag}, rs, 12288 + kvh * 128, 0, SC1);
    __builtin_amdgcn_s_sleep(QDELAY);
    BAR();
    RBAR();
    if (LOOK2) RBAR();
    const u16* wsrc; unsigned wv0, wstep;
    if (j < 16) {
      wsrc = p.wo + (size_t)((kvh * 4 + (j >> 2)) * 128) * 512 + (j & 3) * 128;
      wv0 = g * 1024 + (((c >> 1) ^ g) * 32) + (c & 1) * 16; wstep = 4096;
    } else {
      wsrc = p.w1 + (kvh * 7 + (j - 16)) * 32;
      wv0 = (lane >> 2) * (FF * 2) + (((pp >> 1) ^ (lane >> 5)) * 32) + (pp & 1) * 16; wstep = 16 * FF * 2;
    }
    const unsigned wx = j < 16 ? 128u : 0u;
    const unsigned wv1 = wv0 + wstep, wv2 = (wv0 ^ wx) + 2 * wstep, wv3 = (wv0 ^ wx) + 3 * wstep;
#define W4(i_) do { const int i4 = (i_) ^ ((dph & 7) * 4); const unsigned char* s_ = (const unsigned char*)wsrc + (size_t)i4 * wstep; const unsigned l_ = LB + L_W + i4 * 1024; \
    dma16(s_, wv0, l_); dma16(s_, wv1, l_ + 1024); dma16(s_, wv2, l_ + 2048); dma16(s_, wv3, l_ + 3072); } while (0)
    const bool early = ((unsigned)__builtin_amdgcn_s_getreg(20 | (3 << 11)) & 0xfu) < 4u;
    const int wn1 = j < 16 ? (early ? WN1E : WN1) : (early ? WN1FE : WN1F);
    if (WEARLY && j < 16) {
      __builtin_amdgcn_s_sleep(4);
#pragma unroll 1
      for (int i = 0; i < WEARLY; i += 4) W4(i);
    }
    RBAR();
    npo = __builtin_amdgcn_readfirstlane(LD(unsigned, LB + L_TAG + 44));
    __builtin_amdgcn_s_setprio(0);
    STAMP(4);
    if (j < 23) {
#pragma unroll 1
      for (int i = j < 16 ? WEARLY : 0; i < wn1; i += 4) W4(i);
    }
    {
      const unsigned o_ml = c ? 0x7ffffff0u : pa_ml(kvh, g, b) + sp * 128;
      float mw, lw;
#pragma unroll 1
      for (;;) {
        const unsigned cnt = *(volatile LAS unsigned*)(LB + L_TAG + 4);
        mw = *(volatile LAS float*)(LB + L_MX + g * 16 + pp * 4);
        if (__builtin_amdgcn_readfirstlane(cnt) >= 4u) break;
        __builtin_amdgcn_s_sleep(1);
      }
      float M = vmax(mw, dpp<XOR1>(mw)); M = vmax(M, dpp<XOR2>(M));
      __builtin_amdgcn_raw_buffer_store_b64(v2u{U_(M), tag}, rs, o_ml, npo, SC1);
      const float fw = __builtin_amdgcn_exp2f(mw - M);
#pragma unroll 1
      for (;;) {
        const unsigned cnt = *(volatile LAS unsigned*)(LB + L_TAG + 16);
        lw = *(volatile LAS float*)(LB + L_LS + g * 16 + pp * 4);
        if (__builtin_amdgcn_readfirstlane(cnt) >= 4u) break;
        __builtin_amdgcn_s_sleep(1);
      }
      float S = lw * fw;
      S += dpp<XOR1>(S); S += dpp<XOR2>(S);
      __builtin_amdgcn_raw_buffer_store_b64(v2u{U_(S), tag}, rs, o_ml, npo + 2048, SC1);
    }
    if (j < 23) {
      while (__builtin_amdgcn_readfirstlane(*(volatile LAS unsigned*)(LB + L_TAG + 12)) < 4u) __builtin_amdgcn_s_sleep(1);
      if (FDELAY && early && j >= 16) __builtin_amdgcn_s_sleep(FDELAY);
#pragma unroll 1
      for (int i = wn1; i < 32; i += 4) W4(i);
      if (j >= 16) {
        const u16* src2 = p.w2 + (size_t)((kvh * 7 + (j - 16)) * 32) * 512;
        const unsigned u0 = lane * 16, u1 = (u0 ^ 32u) + 1024u, u2 = (u0 ^ 64u) + 2048u, u3 = (u0 ^ 96u) + 3072u;
#pragma unroll 1
        for (int i = 0; i < 8; ++i) {
          const int i4 = i ^ (dph & 7);
          const unsigned char* s_ = (const unsigned char*)src2 + i4 * 4096; const unsigned l_ = LB + L_W + 32768 + i4 * 4096, x_ = (i4 & 2) * 64;
          dma16(s_, u0 ^ x_, l_); dma16(s_, u1 ^ x_, l_ + 1024); dma16(s_, u2 ^ x_, l_ + 2048); dma16(s_, u3 ^ x_, l_ + 3072);
        }
      }
    }
  } else {
    const int w = wave - 1;
    v4u kf[8], vf[8];
#define LDV(i) LDNT(vp + (pk0 ^ (i)) * 1024 + vo)
    unsigned vo = lane * 16;
    unsigned mk0 = (c & 2) ? 0u : ((c & 1) ? 0xffff0000u : 0x0000ffffu), mk1 = (c & 2) ? ((c & 1) ? 0xffff0000u : 0x0000ffffu) : 0u;
    unsigned a_q = LB + L_QS + (c >> 2) * QS_PITCH + g * 16;
    unsigned a_mw = LB + L_MX + (c >> 2) * 16 + w * 4, a_mr = LB + L_MX + g * 16, a_mo = LB + L_MX + g * 16 + w * 4;
    unsigned a_ow = LB + L_RED + w * RED_PITCH + g * 512 + c * 16;
    unsigned a_or = LB + L_RED + w * 512 + ((lane >> 1) & 1) * 256 + (lane >> 2) * 16 + (lane & 1) * 8;
    unsigned o_pa = pa_data(kvh, w, b) + sp * 512 + lane * 8;
    unsigned a_cnt = LB + L_TAG + 4, one = 1u;
    PIN(vo); PIN(one); PIN(mk0); PIN(mk1); PIN(a_q); PIN(a_mw); PIN(a_mr); PIN(a_mo); PIN(a_ow); PIN(a_or); PIN(o_pa); PIN(a_cnt);
    unsigned a_k = LB + L_K + w * 8192 + (c & 3) * 1024 + (c >> 2) * 256 + ((g ^ c) * 16);
    PIN(a_k);
    if (KS1 < 8) {
      asm volatile("s_waitcnt vmcnt(%0)" ::"i"(KW1) : "memory");
#pragma unroll
      for (int i = KS1; i < KS2; ++i) DMAK(i);
    }
    if (KS2 < 8) {
      asm volatile("s_waitcnt vmcnt(%0)" ::"i"(KW2) : "memory");
#pragma unroll
      for (int i = KS2; i < 8; ++i) DMAK(i);
    }
    if (VW1 >= 0) asm volatile("s_waitcnt vmcnt(%0)" ::"i"(VW1 >= 0 ? VW1 : 0) : "memory");
#pragma unroll
    for (int i = 0; i < NV1; ++i) vf[i] = LDV(i);
    unsigned goff = WS_QG + kvh * 65536 + LD(unsigned, a_cnt + 36) * 8192 + b * 4096 + w * 1024 + lane * 16;
    unsigned a_qw = LB + L_QS + w * QS_PITCH + lane * 4;
    PIN(goff); PIN(a_qw);
    RBAR();
    v4u d[4];
#define QLOAD() _Pragma("unroll") for (int i = 0; i < 4; ++i) d[i] = __builtin_amdgcn_raw_buffer_load_b128(rs, goff, i * 16384, SC1)
    QLOAD();
    RBAR();
#pragma unroll
    for (int i = NV1; i < 8; ++i) vf[i] = LDV(i);
    tag = LD(unsigned, a_cnt - 4);
    asm volatile("s_waitcnt vmcnt(%3) lgkmcnt(0)" : "+s"(pf0), "+s"(pf1), "+s"(pf2) : "i"(LOOK2 ? 16 : 12) : "memory");
#pragma unroll
    for (int i = 0; i < 8; ++i) kf[i] = LD(v4u, (a_k ^ ((i & 3) * 64)) + (i >> 2) * 4096);
#pragma unroll
    for (int i = 0; i < 8; ++i) PIN(kf[i]);
    PIN(tag);
    STAMP2(1);
    __builtin_amdgcn_s_setprio(2);
#define QBAD() ({ unsigned bad = 0; _Pragma("unroll") for (int i = 0; i < 4; ++i) bad = tagbad(bad, d[i], tag); ANY(bad); })
#define QPUT() do { LD(unsigned, a_qw) = pk_bf16((F_(d[0].x) + F_(d[1].x)) + (F_(d[2].x) + F_(d[3].x)), (F_(d[0].z) + F_(d[1].z)) + (F_(d[2].z) + F_(d[3].z))); \
    if (w == 0) asm volatile("s_mov_b64 exec, 1\n\tds_write_b32 %0, %1 offset:40\n\ts_mov_b64 exec, -1" ::"v"(a_cnt), "v"((d[0].x & 1u) << 13) : "memory"); } while (0)
    QPUT();
    if (__builtin_expect(QBAD(), 0)) {
#pragma unroll 1
      for (unsigned spin = 0; spin < SPIN_CAP && QBAD(); ++spin) {
        CBAR();
        goff ^= 8192u;
        QLOAD();
      }
      __builtin_amdgcn_s_waitcnt(0x0F70);
      QPUT();
    }
    BAR();
    STAMP(2);
    v4u qf[4];
#pragma unroll
    for (int s = 0; s < 4; ++s) qf[s] = LD(v4u, a_q + s * 64);
    npo = LD(unsigned, a_cnt + 40);
    __builtin_amdgcn_sched_barrier(0);
    f32x4 sc[2] = {};
#pragma unroll
    for (int s = 0; s < 4; ++s) { sc[0] = mfma(kf[s], qf[s], sc[0]); sc[1] = mfma(kf[4 + s], qf[s], sc[1]); __builtin_amdgcn_sched_barrier(0); }
    o_pa += npo;
    float sv[8];
#pragma unroll
    for (int i = 0; i < 4; ++i) {
      const unsigned r = pk_bf16(sc[i >> 1][(i & 1) * 2], sc[i >> 1][(i & 1) * 2 + 1]);
      const f32x2 t2 = f32x2{bf_lo(r), bf_hi(r)} * 0.08838834764831845f;
      const unsigned t = pk_bf16(t2[0], t2[1]);
      sv[2 * i] = bf_lo(t); sv[2 * i + 1] = bf_hi(t);
    }
    const float mw = xrows_max(vmax3(vmax3(sv[0], sv[1], sv[2]), vmax3(sv[3], sv[4], sv[5]), vmax(sv[6], sv[7]))) * LOG2E;
    asm volatile("s_mov_b64 exec, 0x1111\n\tds_write_b32 %0, %1\n\ts_mov_b64 exec, 1\n\tds_add_u32 %2, %3\n\ts_mov_b64 exec, -1"
                 ::"v"(a_mw), "v"(mw), "v"(a_cnt), "v"(one) : "memory");
    __builtin_amdgcn_sched_barrier(0);
    float e[8];
    f32x4 olo = {}, ohi = {};
    unsigned cnt2; f32x4 m0; float mo;
#pragma unroll
    for (int t = 0; t < 8; ++t) {
      e[t] = __builtin_amdgcn_exp2f(fmaf(sv[t], LOG2E, -mw));
      const unsigned pk = pk_bf16(e[t], e[t]);
      const v2u pa = v2u{pk & mk0, pk & mk1};
      olo = mfma16(pa, v2u{vf[t].x, vf[t].y}, olo); ohi = mfma16(pa, v2u{vf[t].z, vf[t].w}, ohi);
      if (t == 3) {
        __builtin_amdgcn_sched_barrier(0);
        cnt2 = *(volatile LAS unsigned*)(a_cnt); m0 = *(volatile LAS f32x4*)(a_mr); mo = *(volatile LAS float*)(a_mo);
        __builtin_amdgcn_sched_barrier(0);
      }
    }
    float fac = __builtin_amdgcn_exp2f(mo - vmax3(vmax(m0[0], m0[1]), m0[2], m0[3]));
    PIN(fac); PIN(olo); PIN(ohi);
    if (__builtin_expect(__builtin_amdgcn_readfirstlane(cnt2) < 4u, 0)) {
      do {
        cnt2 = *(volatile LAS unsigned*)(a_cnt);
        m0 = *(volatile LAS f32x4*)(a_mr);
      } while (__builtin_amdgcn_readfirstlane(cnt2) < 4u);
      fac = __builtin_amdgcn_exp2f(mo - vmax3(vmax(m0[0], m0[1]), m0[2], m0[3]));
    }
    STAMP2(10);
    LD(f32x4, a_ow) = olo * fac;
    LD(f32x4, a_ow + 256) = ohi * fac;
    asm volatile("s_mov_b64 exec, 1\n\tds_add_u32 %0, %1 offset:4\n\ts_mov_b64 exec, -1" ::"v"(a_cnt), "v"(one) : "memory");
    {
      const float lw = x32_sum(x16_sum(((e[0] + e[1]) + (e[2] + e[3])) + ((e[4] + e[5]) + (e[6] + e[7]))));
      asm volatile("s_mov_b64 exec, 0x1111\n\tds_write_b32 %0, %1 offset:128\n\ts_mov_b64 exec, 1\n\tds_add_u32 %2, %3 offset:12\n\ts_mov_b64 exec, -1"
                   ::"v"(a_mw), "v"(lw), "v"(a_cnt), "v"(one) : "memory");
    }
    STAMP2(4);
    {
      f32x2 r0, r1, r2, r3;
#pragma unroll 1
      for (;;) {
        const unsigned cnt = *(volatile LAS unsigned*)(a_cnt + 4);
        r0 = *(volatile LAS f32x2*)(a_or); r1 = *(volatile LAS f32x2*)(a_or + RED_PITCH);
        r2 = *(volatile LAS f32x2*)(a_or + 2 * RED_PITCH); r3 = *(volatile LAS f32x2*)(a_or + 3 * RED_PITCH);
        if (__builtin_amdgcn_readfirstlane(cnt) >= 4u) break;
      }
      const f32x2 a = (r0 + r1) + (r2 + r3);
      __builtin_amdgcn_raw_buffer_store_b64(v2u{pk_bf16(a[0], a[1]), tag}, rs, o_pa, 0, SC1);
    }
    asm volatile("s_mov_b64 exec, 1\n\tds_add_u32 %0, %1 offset:8\n\ts_mov_b64 exec, -1" ::"v"(a_cnt), "v"(one) : "memory");
    STAMP(5);
  }

  if (j < 16) {
    const int h = kvh * 4 + (j >> 2), cq = j & 3;
    if (wave == 0) {
      if (lane == 0) *(volatile LAS unsigned*)(LB + L_TAG + 28) = 1u;
      STAMP2(7);
      const unsigned mlo = pa_ml(kvh, j >> 2, g & 1) + c * 128 + npo;
      v2u dm;
      asm volatile("buffer_load_dwordx2 %0, %1, %2, 0 offen sc1" : "=v"(dm) : "v"(mlo), "s"(rs) : "memory");
      asm volatile("s_waitcnt vmcnt(1)" ::: "memory");
      if (lane == 0) *(volatile LAS unsigned*)(LB + L_TAG + 20) = 1u;
      STAMP2(9);
      asm volatile("s_waitcnt vmcnt(0)" : "+v"(dm)::"memory");
#pragma unroll 1
      for (unsigned spin = 0; spin < SPIN_CAP && ANY(dm.y ^ tag); ++spin) { CBAR(); dm = __builtin_amdgcn_raw_buffer_load_b64(rs, mlo, 0, SC1); }
      v2u dl = __builtin_amdgcn_raw_buffer_load_b64(rs, mlo, 2048, SC1);
      const float m = F_(dm.x);
      float M = vmax(m, dpp<XOR1>(m)); M = vmax(M, dpp<XOR2>(M)); M = vmax(M, dpp<HMIR>(M)); M = vmax(M, dpp<ROR8>(M));
      const float ex = __builtin_amdgcn_exp2f(m - M);
      const unsigned wb = pk_bf16(ex, ex);
      if (g < 2) *(volatile LAS unsigned*)(LB + L_WN + g * 64 + (c & 3) * 16 + (c >> 2) * 4) = wb;
      if (lane == 0) *(volatile LAS unsigned*)(LB + L_TAG + 24) = 1u;
      STAMP2(8);
#pragma unroll 1
      for (unsigned spin = 0; spin < SPIN_CAP && ANY(dl.y ^ tag); ++spin) { CBAR(); dl = __builtin_amdgcn_raw_buffer_load_b64(rs, mlo, 2048, SC1); }
      float S = bf_lo(wb) * F_(dl.x);
      S += dpp<XOR1>(S); S += dpp<XOR2>(S); S += dpp<HMIR>(S); S += dpp<ROR8>(S);
      if (g < 2 && c == 0) LD(float, LB + L_TAG + 32 + g * 4) = __builtin_amdgcn_rcpf(S);
      BAR();
      STAMP(6);
      STAMP_FLUSH();
      return;
    }
    const int w = wave - 1;
    const int ob = w >> 1, dh = w & 1;
    unsigned off = pa_data(kvh, j >> 2, ob) + g * 512 + dh * 256 + c * 16 + npo;
    v4u d[4];
    unsigned a_ow = LB + L_ACT + ob * ACT_PITCH + dh * 128 + c * 8;
    unsigned a_or = LB + L_ACT + (c & 1) * ACT_PITCH + g * 16;
    unsigned o_pb = g > 1 ? 0x7ffffff0u : pb_inbox(cq * 8 + 2 * w + g) + h * 128 + c * 8;
    unsigned a_wn = LB + L_WN + ob * 64 + g * 16, a_fl = LB + L_TAG + 20, a_inv = LB + L_TAG + 32;
    const int q4 = (lane >> 2) & 3, pp = lane & 3;
    unsigned a_wf = LB + L_W + (8 * g + q4) * 256 + (((2 * w) ^ (q4 | ((g & 1) << 2))) * 32) + pp * 8;
    unsigned mk0 = (c & 2) ? 0u : ((c & 1) ? 0xffff0000u : 0x0000ffffu), mk1 = (c & 2) ? ((c & 1) ? 0xffff0000u : 0x0000ffffu) : 0u;
    PIN(off); PIN(a_ow); PIN(a_or); PIN(o_pb); PIN(a_wn); PIN(a_fl); PIN(a_wf); PIN(a_inv); PIN(mk0); PIN(mk1);
    while (__builtin_amdgcn_readfirstlane(*(volatile LAS unsigned*)(a_fl + 8)) == 0u) __builtin_amdgcn_s_sleep(1);
#define OLOAD() _Pragma("unroll") for (int i = 0; i < 4; ++i) d[i] = __builtin_amdgcn_raw_buffer_load_b128(rs, off + (i & 1) * 2048, (i >> 1) * 4096, SC1)
#define OOK() ({ unsigned bad = 0; _Pragma("unroll") for (int i = 0; i < 4; ++i) bad = tagbad(bad, d[i], tag); !ANY(bad); })
    OLOAD();
    while (__builtin_amdgcn_readfirstlane(*(volatile LAS unsigned*)(a_fl)) == 0u) {}
    CBAR();
    v4u wf[8];
#pragma unroll
    for (int s = 0; s < 8; ++s) wf[s] = tr_frag((a_wf ^ ((s & 1) * 32)) + (s >> 1) * 8192, (a_wf ^ ((s & 1) * 32)) + (s >> 1) * 8192 + 1024);
    v4u wn; unsigned fl;
#pragma unroll 1
    for (unsigned spin = 0; spin < SPIN_CAP; ++spin) {
      fl = *(volatile LAS unsigned*)(a_fl + 4);
      wn = *(volatile LAS v4u*)(a_wn);
      if (OOK()) break;
      CBAR(); OLOAD();
    }
    while (__builtin_amdgcn_readfirstlane(fl) == 0u) {
      fl = *(volatile LAS unsigned*)(a_fl + 4);
      wn = *(volatile LAS v4u*)(a_wn);
    }
    v2u wa[4];
#pragma unroll
    for (int i = 0; i < 4; ++i) wa[i] = v2u{wn[i] & mk0, wn[i] & mk1};
    STAMP(6);
    {
      f32x4 o = {};
#pragma unroll
      for (int i = 0; i < 4; ++i) o = mfma16(wa[i], v2u{d[i].x, d[i].z}, o);
      if (g == 0) LD(v2u, a_ow) = v2u{pk_bf16(o[0], o[1]), pk_bf16(o[2], o[3])};
    }
    BAR();
    f32x4 acc0 = {}, acc1 = {};
    v4u of[4];
#pragma unroll
    for (int s = 0; s < 4; ++s) of[s] = LD(v4u, a_or + s * 64);
    const f32x2 inv = LD(f32x2, a_inv);
    __builtin_amdgcn_sched_barrier(0);
#pragma unroll
    for (int s = 0; s < 4; ++s) { acc0 = mfma(of[s], wf[2 * s], acc0); acc1 = mfma(of[s], wf[2 * s + 1], acc1); }
    __builtin_amdgcn_raw_buffer_store_b64(v2u{pk_bf16((g ? acc1[0] : acc0[0]) * inv[0], (g ? acc1[1] : acc0[1]) * inv[1]), tag}, rs, o_pb, 0, SC1);
    STAMP(7);
  } else if (j < 23) {
    const int f = kvh * 7 + (j - 16);
    if (wave == 0) {
      asm volatile("s_waitcnt vmcnt(0)" ::: "memory");
      STAMP(6);
      RBAR();
      RBAR();
      unsigned a_h = LB + L_H + ((lane >> 2) & 1) * H_PITCH + (lane >> 3) * 32 + (lane & 3) * 8;
      unsigned a_st = LB + L_WN + ((lane >> 2) & 1) * 8;
      PIN(a_h); PIN(a_st);
      v4u d[4];
      poll<4, 1024>(rs, WS_HG + (kvh & 1) * 16384 + npo + ((kvh >> 1) & 1) * 4096 + lane * 16, tag, d);
      STAMP(7);
#pragma unroll
      for (int i = 0; i < 4; ++i) LD(v2u, a_h + i * 256) = v2u{d[i].x, d[i].z};
      BAR();
      PIN(d[0]); PIN(d[1]); PIN(d[2]); PIN(d[3]);
      float s0 = 0.f, s1 = 0.f, q0 = 0.f, q1 = 0.f;
#pragma unroll
      for (int i = 0; i < 4; ++i) {
        s0 = dot2(d[i].x, 0x3f803f80u, s0); s1 = dot2(d[i].z, 0x3f803f80u, s1);
        q0 = dot2(d[i].x, d[i].x, q0); q1 = dot2(d[i].z, d[i].z, q1);
      }
      float t = x16_sum2(s0 + s1, q0 + q1);
      t += dpp<XOR1>(t); t += dpp<XOR2>(t); t += dpp<ROR8>(t);
      t = x32_sum(t) * (1.0f / 512.0f);
      auto u = __builtin_amdgcn_permlane16_swap(U_(t), U_(t), false, false);
      const float mean = F_(u[0]);
      const float var = fmaxf(fmaf(-mean, mean, F_(u[1])), 0.f);
      const float inv = __builtin_amdgcn_rcpf(rbf(__builtin_amdgcn_sqrtf(rbf(rbf(var) + 1e-5f))));
      LD(f32x2, a_st) = f32x2{rbf(mean), inv};
      BAR();
      STAMP(8);
      STAMP_FLUSH();
      return;
    }
    const int w = wave - 1;
    float bias1 = bf_lo(p.b1[f * 32 + (lane & 31)]);
    RBAR();
    v4u w1f[8], w2f[8];
    {
      const int q4 = (lane >> 2) & 3, pp = lane & 3;
      const unsigned b1a = LB + L_W + (128 * w + 8 * g + q4) * 64 + ((g & 1) * 32) + pp * 8;
#pragma unroll
      for (int s = 0; s < 8; ++s) w1f[s] = tr_frag((b1a ^ ((s & 1) * 32)) + (s >> 1) * 2048, (b1a ^ ((s & 1) * 32)) + (s >> 1) * 2048 + 256);
      const unsigned b2a = LB + L_W + 32768 + (8 * g + q4) * 1024 + w * 256 + ((q4 | ((g & 1) << 2)) * 32) + pp * 8;
#pragma unroll
      for (int t = 0; t < 8; ++t) w2f[t] = tr_frag(b2a ^ (t * 32), (b2a ^ (t * 32)) + 4096);
    }
    const f32x4 z4 = {};
    unsigned a_zw = LB + L_ZP + ((g & 1) * 32 + (g >> 1) * 16 + c) * 16 + w * 4;
    unsigned a_zr = LB + L_ZP + lane * 16;
    unsigned a_ha = LB + L_H + (c & 1) * H_PITCH + w * 256 + g * 16;
    unsigned a_st = LB + L_WN + (lane >> 5) * 8;
    unsigned a_gw = LB + L_GA + w * 160 + (lane >> 5) * 80 + (lane & 31) * 2, a_gr = LB + L_GA + w * 160 + (c & 1) * 80 + g * 16;
    unsigned o_pd = WS_PD + (8 * w + g) * 8192 + f * 128 + c * 8;
    float csum;
    {
      const v4u ones = {0x3f803f80u, 0x3f803f80u, 0x3f803f80u, 0x3f803f80u};
      f32x4 c0 = z4, c1 = z4;
#pragma unroll
      for (int s = 0; s < 4; ++s) { c0 = mfma(ones, w1f[2 * s], c0); c1 = mfma(ones, w1f[2 * s + 1], c1); }
      LD(float, a_zw) = (g & 2) ? c1[0] : c0[0];
      BAR();
      const f32x4 a = LD(f32x4, a_zr);
      csum = (a[0] + a[1]) + (a[2] + a[3]);
      asm volatile("" : "+v"(csum), "+v"(w2f[0]), "+v"(w2f[1]), "+v"(w2f[2]), "+v"(w2f[3]), "+v"(w2f[4]), "+v"(w2f[5]), "+v"(w2f[6]), "+v"(w2f[7]), "+v"(bias1));
      PIN(a_zw); PIN(a_zr); PIN(a_ha); PIN(a_st); PIN(a_gw); PIN(a_gr); PIN(o_pd);
    }
    BAR();
    STAMP2(7);
    {
      v4u ha[4];
#pragma unroll
      for (int s = 0; s < 4; ++s) ha[s] = LD(v4u, a_ha + s * 64);
      __builtin_amdgcn_sched_barrier(0);
      f32x4 z0 = z4, z1 = z4;
#pragma unroll
      for (int s = 0; s < 4; ++s) { z0 = mfma(ha[s], w1f[2 * s], z0); z1 = mfma(ha[s], w1f[2 * s + 1], z1); }
      const float t0 = (g & 1) ? z0[1] : z0[0], t1 = (g & 1) ? z1[1] : z1[0];
      LD(float, a_zw) = (g & 2) ? t1 : t0;
    }
    BAR();
    {
      const f32x4 a = LD(f32x4, a_zr);
      const f32x2 st = LD(f32x2, a_st);
      const float zr = (a[0] + a[1]) + (a[2] + a[3]);
      const float z = rbf(rbf(fmaf(-st[0], csum, zr) * st[1]) + bias1);
      const float t = fmaf(z * z, 0.044715f * 0.7978845608028654f * 2.f * LOG2E, 0.7978845608028654f * 2.f * LOG2E);
      const float e = __builtin_amdgcn_exp2f(z * t);
      const float gl = fmaf(-z, __builtin_amdgcn_rcpf(e + 1.0f), z);
      LD(u16, a_gw) = (u16)pk_bf16(gl, gl);
    }
    CBAR();
    const v4u ga = LD(v4u, a_gr);
#pragma unroll
    for (int u = 0; u < 2; ++u) {
      const f32x4 y0 = mfma(ga, w2f[4 * u], z4), y1 = mfma(ga, w2f[4 * u + 1], z4), y2 = mfma(ga, w2f[4 * u + 2], z4), y3 = mfma(ga, w2f[4 * u + 3], z4);
      const unsigned p0 = pk_bf16(y0[0], y0[1]), p1 = pk_bf16(y1[0], y1[1]), p2 = pk_bf16(y2[0], y2[1]), p3 = pk_bf16(y3[0], y3[1]);
      __builtin_amdgcn_raw_buffer_store_b64(v2u{(g & 2) ? ((g & 1) ? p3 : p2) : ((g & 1) ? p1 : p0), tag}, rs, o_pd, u * 4 * 8192, SC1);
    }
    STAMP(8);
  } else if (j < 31 && wave == 0) {
    const unsigned np = npo >> 13;
    const unsigned role = claim_role(p.ws, (((j - 23) * 4 + (kvh & 3)) * 2) | np, tag);
    const int r = role & 31, n = lane & 7, pg = lane >> 3;
    if (j < 27) {
      unsigned xr = *(const unsigned*)(p.x + (g & 1) * 512 + r * 16 + 2 * n);
      unsigned o_hg = WS_HG + ((lane >> 3) & 1) * 8192 + (g >> 1) * 4096 + r * 128 + ((g & 1) * 8 + n) * 8;
      PIN(xr); PIN(o_hg);
      STAMP2(9);
      v4u d[4];
      poll<4, 1024>(rs, pb_inbox(r) + lane * 16, tag, d);
      STAMP(6);
      const float a0 = (bf_lo(d[0].x) + bf_lo(d[1].x)) + (bf_lo(d[2].x) + bf_lo(d[3].x)), a1 = (bf_hi(d[0].x) + bf_hi(d[1].x)) + (bf_hi(d[2].x) + bf_hi(d[3].x));
      const float b0 = (bf_lo(d[0].z) + bf_lo(d[1].z)) + (bf_lo(d[2].z) + bf_lo(d[3].z)), b1 = (bf_hi(d[0].z) + bf_hi(d[1].z)) + (bf_hi(d[2].z) + bf_hi(d[3].z));
      float t = x32_sum2(x16_sum2(a0, a1), x16_sum2(b0, b1));
      t += dpp<ROR8>(t);
      auto sw = __builtin_amdgcn_permlane32_swap(U_(t), U_(t), false, false);
      const unsigned ar = pk_bf16(F_(sw[0]), F_(sw[1]));
      const unsigned pk = pk_bf16(bf_lo(xr) + bf_lo(ar), bf_hi(xr) + bf_hi(ar));
      __builtin_amdgcn_raw_buffer_store_b64(v2u{pk, tag}, rs, o_hg, 0, SC1);
      __builtin_amdgcn_raw_buffer_store_b64(v2u{pk, tag}, rs, o_hg, 16384, SC1);
      __builtin_amdgcn_raw_buffer_store_b64(v2u{pk, tag}, rs, o_hg, 32768, SC1);
      if (r == 0 && lane == 0) __builtin_amdgcn_raw_buffer_store_b32(tag, rs, WS_EPOCH, 0, SC1);
      STAMP(7);
    } else {
      unsigned b2r = *(const unsigned*)(p.b2 + r * 16 + 2 * n);
      v2u hg;
      const unsigned hoff = WS_HG + 32768 + np * 8192 + r * 128 + ((g & 1) * 8 + n) * 8;
#pragma unroll 1
      for (unsigned spin = 0; spin < SPIN_CAP; ++spin) {
        CBAR();
        hg = __builtin_amdgcn_raw_buffer_load_b64(rs, hoff, 0, SC1);
        if (__all(hg.y == tag)) break;
        __builtin_amdgcn_s_sleep(2);
      }
      unsigned o_out = ((g & 1) * 512 + r * 16 + 2 * n) * 2;
      PIN(b2r); PIN(o_out);
      if (RDDELAY) __builtin_amdgcn_s_sleep(RDDELAY);
      v4u d[7];
      poll<7, 1024>(rs, WS_PD + r * 8192 + lane * 16, tag, d);
      STAMP(8);
#define S7(f, m) (((f(d[0].m) + f(d[1].m)) + (f(d[2].m) + f(d[3].m))) + ((f(d[4].m) + f(d[5].m)) + f(d[6].m)))
      float t = x32_sum2(x16_sum2(S7(bf_lo, x), S7(bf_hi, x)), x16_sum2(S7(bf_lo, z), S7(bf_hi, z)));
      t += dpp<ROR8>(t);
      auto sw = __builtin_amdgcn_permlane32_swap(U_(t), U_(t), false, false);
      const unsigned ar = pk_bf16(F_(sw[0]), F_(sw[1]));
      const unsigned yr = pk_bf16(bf_lo(ar) + bf_lo(b2r), bf_hi(ar) + bf_hi(b2r));
      const unsigned pk = pk_bf16(bf_lo(hg.x) + bf_lo(yr), bf_hi(hg.x) + bf_hi(yr));
      if ((lane & 40) == 0) *(unsigned*)((unsigned char*)p.out + o_out) = pk;
      if (r == 0) {
        const unsigned tend = (unsigned)__builtin_amdgcn_s_memrealtime();
        const v2u sv = __builtin_amdgcn_raw_buffer_load_b64(rs, 12288 + (lane & 7) * 128, 0, SC1);
        int mn = 0x7fffffff, mx = -0x7fffffff;
        for (int k = 0; k < 8; ++k) { const int t = (int)(__builtin_amdgcn_readlane(sv.x, k) - tend); mn = t < mn ? t : mn; mx = t > mx ? t : mx; }
        const unsigned dur = 100u;
        while ((unsigned)__builtin_amdgcn_s_memrealtime() - tend < dur) __builtin_amdgcn_s_sleep(1);
      }
    }
  }
  STAMP_FLUSH();
}

}

extern "C" void kernel_launch(void* const* d_in, const int* in_sizes, int n_in,
                              void* d_out, int out_size, void* d_ws, size_t ws_size,
                              hipStream_t stream) {
  Params p;
  p.x = (const u16*)d_in[0]; p.kc = (const u16*)d_in[1]; p.vc = (const u16*)d_in[2];
  p.wq = (const u16*)d_in[3]; p.wo = (const u16*)d_in[4];
  p.w1 = (const u16*)d_in[6]; p.b1 = (const u16*)d_in[7]; p.w2 = (const u16*)d_in[8]; p.b2 = (const u16*)d_in[9];
  p.out = (u16*)d_out; p.ws = (unsigned char*)(((uintptr_t)d_ws + 16383) & ~(uintptr_t)16383);
  (void)hipFuncSetAttribute((const void*)fused_decode, hipFuncAttributeMaxDynamicSharedMemorySize, LDS_BYTES);
  hipLaunchKernelGGL(fused_decode, dim3(256), dim3(NTHR), LDS_BYTES, stream, p);
}
```

```cpp
#include <hip/hip_runtime.h>
#include <hip/hip_bf16.h>
#include <stdint.h>

typedef unsigned short u16;
typedef unsigned long long u64;
typedef unsigned v4u __attribute__((ext_vector_type(4)));
typedef unsigned v2u __attribute__((ext_vector_type(2)));
typedef float f32x4 __attribute__((ext_vector_type(4)));
typedef float f32x2 __attribute__((ext_vector_type(2)));
typedef short bf16x8 __attribute__((ext_vector_type(8)));
typedef short bf16x4 __attribute__((ext_vector_type(4)));
typedef __bf16 bf16x2 __attribute__((ext_vector_type(2)));
typedef short v4i16 __attribute__((ext_vector_type(4)));
#define LAS __attribute__((address_space(3)))
#define GAS __attribute__((address_space(1)))

namespace {

constexpr int SK = 8192, WIN = 2048, FF = 1792;
constexpr int NTHR = 320;
#define QDELAY 36
#ifndef NV0
#define NV0 1
#endif
#ifndef NV1
#define NV1 5
#endif
#define WN1F 16
#define CODE_BYTES 14144
constexpr unsigned SPIN_CAP = 1u << 18;
constexpr float LOG2E = 1.4426950408889634f;

constexpr unsigned WS_EPOCH = 0;
constexpr unsigned WS_CLAIM = 1024;
constexpr unsigned WS_CAL = 16384;
constexpr unsigned WS_HG = WS_CAL + 65536;
constexpr unsigned WS_PB = WS_HG + 3 * 16384;
constexpr unsigned WS_PD = WS_PB + 8 * 16384;
constexpr unsigned WS_PA = WS_PD + 32 * 8192;
constexpr unsigned WS_QG = WS_PA + 8 * 9 * 16384;
constexpr unsigned WS_END = WS_QG + 8 * 4 * 8192;
__host__ __device__ constexpr unsigned pa_data(unsigned kvh, unsigned hh, unsigned b) { return WS_PA + (kvh * 9 + hh * 2 + b) * 16384; }
__host__ __device__ constexpr unsigned pa_ml(unsigned kvh, unsigned hh, unsigned b) { return WS_PA + (kvh * 9 + 8) * 16384 + b * 4096 + hh * 8; }
constexpr unsigned WS_STAMP = 4u << 20;
__host__ __device__ constexpr unsigned pb_inbox(unsigned r) { return WS_PB + (r >> 2) * 16384 + (r & 1) * 8192 + ((r >> 1) & 1) * 4096; }

constexpr int L_WQ = 0;
constexpr int RED_PITCH = 2048;
constexpr int L_RED = 0;
constexpr int L_W = 17408;
constexpr int L_K = L_W + 32768;
constexpr int L_ACT = L_W + 65536;
constexpr int ACT_PITCH = 256 + 64;
constexpr int L_QS = L_ACT + 2 * ACT_PITCH;
constexpr int QS_PITCH = 256 + 64;
constexpr int L_MX = L_QS + 4 * QS_PITCH;
constexpr int L_LS = L_MX + 128;
constexpr int L_WN = L_LS + 128;
constexpr int L_ZP = L_WN + 128;
constexpr int L_GA = L_ZP + 64 * 48;
constexpr int L_H = L_GA + 8 * 160;
constexpr int H_PITCH = 1024 + 64;
constexpr int L_TAG = L_H + 2 * H_PITCH;
constexpr int LDS_BYTES = L_TAG + 64;
static_assert(8 * RED_PITCH <= L_W, "lds map");
static_assert(LDS_BYTES > 81920, "one workgroup per CU");

struct Params {
  const u16* x; const u16* kc; const u16* vc; const u16* wq; const u16* wo;
  const u16* w1; const u16* b1; const u16* w2; const u16* b2;
  u16* out; unsigned char* ws;
};

#define F_(v) __uint_as_float(v)
#define U_(v) __float_as_uint(v)
__device__ __forceinline__ float bf_lo(unsigned d) { return F_(d << 16); }
__device__ __forceinline__ float bf_hi(unsigned d) { return F_(d & 0xffff0000u); }
__device__ __forceinline__ unsigned pk_bf16(float lo, float hi) {
  return __builtin_bit_cast(unsigned, __builtin_convertvector(f32x2{lo, hi}, bf16x2));
}
__device__ __forceinline__ float rbf(float f) { return bf_lo(pk_bf16(f, f)); }
__device__ __forceinline__ f32x4 mfma(v4u a, v4u b, f32x4 c) {
  return __builtin_amdgcn_mfma_f32_16x16x32_bf16(__builtin_bit_cast(bf16x8, a), __builtin_bit_cast(bf16x8, b), c, 0, 0, 0);
}
__device__ __forceinline__ f32x4 mfma16(v2u a, v2u b, f32x4 c) {
  return __builtin_amdgcn_mfma_f32_16x16x16bf16_1k(__builtin_bit_cast(bf16x4, a), __builtin_bit_cast(bf16x4, b), c, 0, 0, 0);
}
__device__ __forceinline__ float dot2(unsigned a, unsigned b, float acc) {
  return __builtin_amdgcn_fdot2_f32_bf16(__builtin_bit_cast(bf16x2, a), __builtin_bit_cast(bf16x2, b), acc, false);
}
template <int CTRL> __device__ __forceinline__ float dpp(float x) {
  return __builtin_bit_cast(float, __builtin_amdgcn_mov_dpp(__builtin_bit_cast(int, x), CTRL, 0xf, 0xf, true));
}
constexpr int XOR1 = 0xB1, XOR2 = 0x4E, HMIR = 0x141, ROR8 = 0x128;
__device__ __forceinline__ float x16_sum(float x) {
  auto s = __builtin_amdgcn_permlane16_swap(U_(x), U_(x), false, false);
  return F_(s[0]) + F_(s[1]);
}
__device__ __forceinline__ float x32_sum(float x) {
  auto s = __builtin_amdgcn_permlane32_swap(U_(x), U_(x), false, false);
  return F_(s[0]) + F_(s[1]);
}
__device__ __forceinline__ float x16_sum2(float a, float b) {
  auto s = __builtin_amdgcn_permlane16_swap(U_(a), U_(b), false, false);
  return F_(s[0]) + F_(s[1]);
}
__device__ __forceinline__ float x32_sum2(float a, float b) {
  auto s = __builtin_amdgcn_permlane32_swap(U_(a), U_(b), false, false);
  return F_(s[0]) + F_(s[1]);
}
__device__ __forceinline__ float vmax(float a, float b) { float r; asm("v_max_f32 %0, %1, %2" : "=v"(r) : "v"(a), "v"(b)); return r; }
__device__ __forceinline__ float vmax3(float a, float b, float c) { float r; asm("v_max3_f32 %0, %1, %2, %3" : "=v"(r) : "v"(a), "v"(b), "v"(c)); return r; }
__device__ __forceinline__ float xrows_max(float x) {
  auto s = __builtin_amdgcn_permlane16_swap(U_(x), U_(x), false, false);
  x = vmax(F_(s[0]), F_(s[1]));
  auto t = __builtin_amdgcn_permlane32_swap(U_(x), U_(x), false, false);
  return vmax(F_(t[0]), F_(t[1]));
}
#define BAR() asm volatile("s_waitcnt lgkmcnt(0)\n\ts_barrier" ::: "memory")
#define RBAR() asm volatile("s_barrier" ::: "memory")
#define CBAR() asm volatile("" ::: "memory")
#define PIN(x) asm volatile("" : "+v"(x))
#define LDNT(p) __builtin_nontemporal_load((const v4u*)(p))
#define SC1 16
#define LD(T, a) (*(LAS T*)(a))

__device__ __forceinline__ void dma16(const void* sbase, unsigned voff, unsigned ldsaddr) {
  asm volatile("s_mov_b32 m0, %2\n\ts_nop 0\n\tglobal_load_lds_dwordx4 %0, %1 nt" ::"v"(voff), "s"(sbase), "s"(ldsaddr) : "memory");
}
__device__ __forceinline__ v4u tr_frag(unsigned a0, unsigned a1) {
  v2u a = __builtin_bit_cast(v2u, __builtin_amdgcn_ds_read_tr16_b64_v4i16((LAS v4i16*)a0));
  v2u b = __builtin_bit_cast(v2u, __builtin_amdgcn_ds_read_tr16_b64_v4i16((LAS v4i16*)a1));
  return v4u{a.x, a.y, b.x, b.y};
}
__device__ __forceinline__ v4u ldg(__amdgpu_buffer_rsrc_t rs, unsigned off) { return __builtin_amdgcn_raw_buffer_load_b128(rs, off, 0, SC1); }

__device__ __forceinline__ void cal_issue(const unsigned char* ws) {
  const unsigned o = WS_CAL + (blockIdx.x >> 6) * 16384 + (blockIdx.x & 63) * 128;
  asm volatile("s_mov_b64 exec, 1\n\tglobal_store_dword %0, %0, %2 sc1\n\tglobal_store_dword %1, %0, %2 sc1\n\ts_mov_b64 exec, -1"
               ::"v"(o), "v"(o + 8192), "s"(ws) : "memory");
}
__device__ __forceinline__ unsigned cal_result() {
  u64 t1, t2;
  asm volatile("s_waitcnt vmcnt(1)\n\ts_memtime %0\n\ts_waitcnt vmcnt(0)\n\ts_memtime %1\n\ts_waitcnt lgkmcnt(0)" : "=&s"(t1), "=&s"(t2)::"memory");
  return ((unsigned)t2 - (unsigned)t1) > 100u ? 0u : 1u;
}
__device__ __forceinline__ unsigned claim_role(unsigned char* ws, unsigned role, unsigned tag) {
  unsigned old = 0;
  if ((threadIdx.x & 63) == 0) old = __hip_atomic_exchange((GAS unsigned*)(ws + WS_CLAIM + role * 128), tag, __ATOMIC_RELAXED, __HIP_MEMORY_SCOPE_AGENT);
  return __builtin_amdgcn_readfirstlane(old) == tag ? role ^ 1u : role;
}

__device__ __forceinline__ unsigned tagbad(unsigned bad, v4u d, unsigned tag) {
  return __builtin_amdgcn_bitop3_b32(__builtin_amdgcn_bitop3_b32(bad, d.y, tag, 0xF6), d.w, tag, 0xF6);
}
#define ANY(bad) (__builtin_amdgcn_ballot_w64((bad) != 0u) != 0ull)
template <int N, int STRIDE>
__device__ __forceinline__ void poll(__amdgpu_buffer_rsrc_t rs, unsigned off, unsigned tag, v4u (&d)[N]) {
  unsigned n = SPIN_CAP;
#pragma unroll 1
  for (;;) {
    asm volatile("" : "+s"(n)::"memory");
#pragma unroll
    for (int i = 0; i < N; ++i) d[i] = __builtin_amdgcn_raw_buffer_load_b128(rs, off + (i & 3) * STRIDE, (i >> 2) * 4 * STRIDE, SC1);
    unsigned bad = 0;
#pragma unroll
    for (int i = 0; i < N; ++i) bad = tagbad(bad, d[i], tag);
    if (!ANY(bad)) break;
    if (--n == 0u) break;
  }
}

template <int N>
__device__ __forceinline__ f32x4 poll_sum(__amdgpu_buffer_rsrc_t rs, unsigned off, unsigned tag) {
  unsigned n = SPIN_CAP, klo = 0x00003f80u, khi = 0x3f800000u;
  PIN(klo); PIN(khi);
  f32x4 s;
#pragma unroll 1
  for (;;) {
    asm volatile("" : "+s"(n)::"memory");
    v4u d[N];
#pragma unroll
    for (int i = 0; i < N; ++i) d[i] = __builtin_amdgcn_raw_buffer_load_b128(rs, off + (i & 3) * 1024, (i >> 2) * 4096, SC1);
    unsigned bad = 0;
    s = f32x4{0.f, 0.f, 0.f, 0.f};
#pragma unroll
    for (int i = 0; i < N; ++i) {
      s[0] = dot2(d[i].x, klo, s[0]); s[1] = dot2(d[i].x, khi, s[1]);
      s[2] = dot2(d[i].z, klo, s[2]); s[3] = dot2(d[i].z, khi, s[3]);
      bad = tagbad(bad, d[i], tag);
    }
    PIN(s);
    if (!ANY(bad)) break;
    if (--n == 0u) break;
  }
  return s;
}
__device__ __forceinline__ float half_sum(float x) {
  x += dpp<XOR1>(x); x += dpp<XOR2>(x); x += dpp<HMIR>(x); x += dpp<ROR8>(x);
  return x16_sum(x);
}
__device__ __forceinline__ void ln_stats(v4u a, v4u b, float& mb, float& inv) {
  float s0 = 0.f, s1 = 0.f;
#pragma unroll
  for (int i = 0; i < 4; ++i) { s0 = dot2(a[i], 0x3f803f80u, s0); s1 = dot2(b[i], 0x3f803f80u, s1); }
  const float mean = half_sum(s0 + s1) * (1.0f / 512.0f);
  f32x2 q0 = {0.f, 0.f}, q1 = {0.f, 0.f};
#pragma unroll
  for (int i = 0; i < 4; ++i) {
    const f32x2 u = f32x2{bf_lo(a[i]), bf_hi(a[i])} - mean, v = f32x2{bf_lo(b[i]), bf_hi(b[i])} - mean;
    q0 = u * u + q0; q1 = v * v + q1;
  }
  q0 += q1;
  const float var = half_sum(q0[0] + q0[1]) * (1.0f / 512.0f);
  mb = rbf(mean);
  inv = __builtin_amdgcn_rcpf(rbf(__builtin_amdgcn_sqrtf(rbf(rbf(var) + 1e-5f))));
}

#define STAMP(i) do {} while (0)
#define STAMP_DECL
#define STAMP_FLUSH() do {} while (0)
#define STAMP2(i) do {} while (0)

__global__ void __launch_bounds__(NTHR, 1) fused_decode(Params p) {
  extern __shared__ __attribute__((aligned(16))) unsigned char lds_raw[];
  u64 pc;
  asm volatile("s_getpc_b64 %0" : "=s"(pc));
  const unsigned LB = (unsigned)(size_t)(LAS unsigned char*)lds_raw;
  const int tid = threadIdx.x, lane = tid & 63, hwave = __builtin_amdgcn_readfirstlane(tid >> 6);
  const int wave = hwave == 4 ? 0 : hwave + 1;
  const int kvh = blockIdx.x & 7, j = blockIdx.x >> 3;
  const int g = lane >> 4, c = lane & 15;
  STAMP_DECL;
  STAMP(0);
  const __amdgpu_buffer_rsrc_t rs = __builtin_amdgcn_make_buffer_rsrc((void*)p.ws, 0, WS_END, 0x00020000);
  const int b = j >> 4, sp = j & 15;
  unsigned tag, npo;

  const int rg = j >> 3, cg = j & 7;
  const int dph = j + kvh * 5;
  unsigned ep; v4u x0, x1; v2u xs;
  asm volatile("" ::"s"(p.ws), "s"(p.kc), "s"(p.out));
  unsigned pf0 = 0, pf1 = 0, pf2 = 0;
  if (wave > 1) {
    unsigned o = (j * 3 + wave - 2) * 192;
    o = o < CODE_BYTES - 192 ? o : CODE_BYTES - 192;
    asm volatile("s_load_dword %0, %3, 0x0\n\ts_load_dword %1, %3, 0x40\n\ts_load_dword %2, %3, 0x80" : "=&s"(pf0), "=&s"(pf1), "=&s"(pf2) : "s"((pc & ~255ull) + o), "s"(p.ws), "s"(p.kc), "s"(p.out));
  }
  if (wave == 1) { cal_issue(p.ws); const unsigned np0 = cal_result(); if (lane == 0) LD(unsigned, LB + L_TAG + 40) = np0; asm volatile("s_waitcnt lgkmcnt(0)" ::: "memory"); }
  if (wave == 0) {
    __builtin_amdgcn_s_setprio(3);
    asm volatile("global_load_dword %0, %1, %2 sc1" : "=v"(ep) : "v"(WS_EPOCH), "s"(p.ws) : "memory");
    asm volatile("global_load_dwordx4 %0, %1, %2" : "=v"(x0) : "v"(lane * 32), "s"(p.x) : "memory");
    asm volatile("global_load_dwordx4 %0, %1, %2 offset:16" : "=v"(x1) : "v"(lane * 32), "s"(p.x) : "memory");
    asm volatile("global_load_dwordx2 %0, %1, %2" : "=v"(xs) : "v"((lane >> 5) * 1024 + rg * 256 + (lane & 31) * 8), "s"(p.x) : "memory");
    const int r0 = lane >> 3, pos = lane & 7;
    const unsigned v0 = r0 * 8192 + (((pos >> 1) ^ ((r0 >> 1) & 1)) * 32) + (pos & 1) * 16;
    const u16* src = p.wq + (size_t)(rg * 128) * 4096 + kvh * 512 + cg * 64;
    const unsigned v1 = (v0 ^ 64u) + 65536u, va = (dph & 8) ? v1 : v0, vb = (dph & 8) ? v0 : v1, la = (dph & 8) ? 1024u : 0u;
#pragma unroll 1
    for (int i = 0; i < 8; ++i) {
      const int ii = i ^ (dph & 7);
      dma16(src + (size_t)ii * 16 * 4096, va, LB + L_WQ + ii * 2048 + la);
      dma16(src + (size_t)ii * 16 * 4096, vb, LB + L_WQ + ii * 2048 + (la ^ 1024));
    }
  }
  const size_t kvbase = ((size_t)(b * 8 + kvh) * SK + (SK - WIN) + sp * 128) * 128;
  const unsigned char* kp = (const unsigned char*)(p.kc + kvbase);
  const unsigned char* vp = (const unsigned char*)(p.vc + kvbase);
  const int pk0 = (((wave - 1) * 8) ^ dph) & 31;
  unsigned ko = g * 256 + ((c ^ (4 * g)) * 16);
  PIN(ko);
  RBAR();
  {
    int wave1 = wave;
    asm volatile("" : "+s"(wave1));
    if (wave1 != 0) {
      const int w = wave - 1;
#define DMAK(i) dma16(kp + (pk0 ^ (i)) * 1024, ko ^ (((i) & 3) * 16), LB + L_K + w * 8192 + (i) * 1024)
#pragma unroll
      for (int i = 0; i < 8; ++i) DMAK(i);
    }
  }
  int wave2 = wave;
  asm volatile("" : "+s"(wave2));

  if (__builtin_expect(wave2 == 0, 1)) {
    if (lane < 8) LD(unsigned, LB + L_TAG + 4 + lane * 4) = 0u;
    if (lane == 8) LD(unsigned, LB + L_TAG + 48) = 0u;
    const int q4 = (lane >> 2) & 3, pp = lane & 3;
    unsigned tra = LB + L_WQ + (8 * g + q4) * 128 + pp * 8 + ((((q4 >> 1) & 1) | ((g & 1) << 1)) * 32) + ((dph >> 1) & 3) * 4096;
    unsigned qoff = WS_QG + (kvh * 4 + rg) * 8192 + (cg * 64 + lane) * 8;
    unsigned a_ln = LB + L_ACT + (lane >> 5) * ACT_PITCH + (lane & 31) * 8;
    unsigned a_af = LB + L_ACT + (c & 1) * ACT_PITCH + ((dph >> 1) & 3) * 64;
    unsigned af0 = a_af + g * 16, af1 = (((dph >> 1) & 3) ^ 1) * 64 + LB + L_ACT + (c & 1) * ACT_PITCH + g * 16;
    unsigned af2 = (((dph >> 1) & 3) ^ 2) * 64 + LB + L_ACT + (c & 1) * ACT_PITCH + g * 16, af3 = (((dph >> 1) & 3) ^ 3) * 64 + LB + L_ACT + (c & 1) * ACT_PITCH + g * 16;
    unsigned trs[16];
#pragma unroll
    for (int i = 0; i < 16; ++i) { trs[i] = tra ^ ((i & 3) * 32 + (i >> 2) * 4096); PIN(trs[i]); }
    PIN(qoff); PIN(a_ln); PIN(af0); PIN(af1); PIN(af2); PIN(af3);
    asm volatile("s_waitcnt vmcnt(16)" : "+v"(ep), "+v"(x0), "+v"(x1), "+v"(xs)::"memory");
    STAMP2(1);
    v4u af[4];
    {
      float mb, inv;
      ln_stats(x0, x1, mb, inv);
      const unsigned c0 = pk_bf16(bf_lo(xs.x) - mb, bf_hi(xs.x) - mb), c1 = pk_bf16(bf_lo(xs.y) - mb, bf_hi(xs.y) - mb);
      LD(v2u, a_ln) = v2u{pk_bf16(bf_lo(c0) * inv, bf_hi(c0) * inv), pk_bf16(bf_lo(c1) * inv, bf_hi(c1) * inv)};
      CBAR();
      af[0] = LD(v4u, af0); af[1] = LD(v4u, af1); af[2] = LD(v4u, af2); af[3] = LD(v4u, af3);
      CBAR();
    }
    tag = (__builtin_amdgcn_readfirstlane(ep) | 1u) + 1u;
    if (lane == 0) LD(unsigned, LB + L_TAG) = tag;
    const unsigned np0 = LD(unsigned, LB + L_TAG + 40);
    STAMP2(5);
    f32x4 acc[4] = {};
#pragma unroll
    for (int s = 0; s < 4; ++s) {
      if (s == 0) asm volatile("s_waitcnt vmcnt(12)" ::: "memory");
      if (s == 1) asm volatile("s_waitcnt vmcnt(8)" ::: "memory");
      if (s == 2) asm volatile("s_waitcnt vmcnt(4)" ::: "memory");
      if (s == 3) { asm volatile("s_waitcnt vmcnt(0)" ::: "memory"); STAMP2(2); }
      v4u bf[4];
#pragma unroll
      for (int t = 0; t < 4; ++t) bf[t] = tr_frag(trs[4 * s + t], trs[4 * s + t] + 512);
      __builtin_amdgcn_sched_barrier(0);
#pragma unroll
      for (int t = 0; t < 4; ++t) acc[t] = mfma(af[s], bf[t], acc[t]);
      __builtin_amdgcn_sched_barrier(0);
    }
    {
      const float s0 = (g & 1) ? acc[1][0] : acc[0][0], s1 = (g & 1) ? acc[1][1] : acc[0][1];
      const float s2 = (g & 1) ? acc[3][0] : acc[2][0], s3 = (g & 1) ? acc[3][1] : acc[2][1];
      const v2u q0 = {U_((g & 2) ? s2 : s0), tag | np0}, q1 = {U_((g & 2) ? s3 : s1), tag | np0};
      __builtin_amdgcn_raw_buffer_store_b64(q0, rs, qoff, 0, SC1);
      __builtin_amdgcn_raw_buffer_store_b64(q1, rs, qoff, 4096, SC1);
    }
    STAMP(3);
    RBAR();
    __builtin_amdgcn_s_sleep(QDELAY);
    BAR();
    RBAR();
    const u16* wsrc; unsigned wv0, wstep;
    if (j < 16) {
      wsrc = p.wo + (size_t)((kvh * 4 + (j >> 2)) * 128) * 512 + (j & 3) * 128;
      wv0 = g * 1024 + (((c >> 1) ^ g) * 32) + (c & 1) * 16; wstep = 4096;
    } else {
      wsrc = p.w1 + (kvh * 7 + (j - 16)) * 32;
      wv0 = (lane >> 2) * (FF * 2) + (((pp >> 1) ^ (lane >> 5)) * 32) + (pp & 1) * 16; wstep = 16 * FF * 2;
    }
    const unsigned wx = j < 16 ? 128u : 0u;
    const unsigned wv1 = wv0 + wstep, wv2 = (wv0 ^ wx) + 2 * wstep, wv3 = (wv0 ^ wx) + 3 * wstep;
#define W4(i_) do { const int i4 = (i_) ^ ((dph & 7) * 4); const unsigned char* s_ = (const unsigned char*)wsrc + (size_t)i4 * wstep; const unsigned l_ = LB + L_W + i4 * 1024; \
    dma16(s_, wv0, l_); dma16(s_, wv1, l_ + 1024); dma16(s_, wv2, l_ + 2048); dma16(s_, wv3, l_ + 3072); } while (0)
    const int wn1 = j < 16 ? 32 : WN1F;
    RBAR();
    npo = __builtin_amdgcn_readfirstlane(LD(unsigned, LB + L_TAG + 44));
    __builtin_amdgcn_s_setprio(0);
    STAMP(4);
    if (j < 23) {
#pragma unroll 1
      for (int i = 0; i < wn1; i += 4) W4(i);
    }
    {
      const unsigned o_ml = c ? 0x7ffffff0u : pa_ml(kvh, g, b) + sp * 128;
      float mw, lw;
#pragma unroll 1
      for (;;) {
        const unsigned cnt = *(volatile LAS unsigned*)(LB + L_TAG + 4);
        mw = *(volatile LAS float*)(LB + L_MX + g * 16 + pp * 4);
        if (__builtin_amdgcn_readfirstlane(cnt) >= 4u) break;
        __builtin_amdgcn_s_sleep(1);
      }
      float M = vmax(mw, dpp<XOR1>(mw)); M = vmax(M, dpp<XOR2>(M));
      __builtin_amdgcn_raw_buffer_store_b64(v2u{U_(M), tag}, rs, o_ml, npo, SC1);
      const float fw = __builtin_amdgcn_exp2f(mw - M);
#pragma unroll 1
      for (;;) {
        const unsigned cnt = *(volatile LAS unsigned*)(LB + L_TAG + 16);
        lw = *(volatile LAS float*)(LB + L_LS + g * 16 + pp * 4);
        if (__builtin_amdgcn_readfirstlane(cnt) >= 4u) break;
        __builtin_amdgcn_s_sleep(1);
      }
      float S = lw * fw;
      S += dpp<XOR1>(S); S += dpp<XOR2>(S);
      __builtin_amdgcn_raw_buffer_store_b64(v2u{U_(S), tag}, rs, o_ml, npo + 2048, SC1);
    }
    if (j >= 16 && j < 23) {
      while (__builtin_amdgcn_readfirstlane(*(volatile LAS unsigned*)(LB + L_TAG + 12)) < 4u) __builtin_amdgcn_s_sleep(1);
#pragma unroll 1
      for (int i = WN1F; i < 32; i += 4) W4(i);
      {
        const u16* src2 = p.w2 + (size_t)((kvh * 7 + (j - 16)) * 32) * 512;
        const unsigned u0 = lane * 16, u1 = (u0 ^ 32u) + 1024u, u2 = (u0 ^ 64u) + 2048u, u3 = (u0 ^ 96u) + 3072u;
#pragma unroll 1
        for (int i = 0; i < 8; ++i) {
          const int i4 = i ^ (dph & 7);
          const unsigned char* s_ = (const unsigned char*)src2 + i4 * 4096; const unsigned l_ = LB + L_W + 32768 + i4 * 4096, x_ = (i4 & 2) * 64;
          dma16(s_, u0 ^ x_, l_); dma16(s_, u1 ^ x_, l_ + 1024); dma16(s_, u2 ^ x_, l_ + 2048); dma16(s_, u3 ^ x_, l_ + 3072);
        }
      }
    }
  } else {
    const int w = wave - 1;
    v4u kf[8], vf[8];
#define LDV(i) LDNT(vp + (pk0 ^ (i)) * 1024 + vo)
    unsigned vo = lane * 16;
    unsigned mk0 = (c & 2) ? 0u : ((c & 1) ? 0xffff0000u : 0x0000ffffu), mk1 = (c & 2) ? ((c & 1) ? 0xffff0000u : 0x0000ffffu) : 0u;
    unsigned a_q = LB + L_QS + (c >> 2) * QS_PITCH + g * 16;
    unsigned a_mw = LB + L_MX + (c >> 2) * 16 + w * 4, a_mr = LB + L_MX + g * 16, a_mo = LB + L_MX + g * 16 + w * 4;
    unsigned a_ow = LB + L_RED + w * RED_PITCH + g * 512 + c * 16;
    unsigned a_or = LB + L_RED + w * 512 + ((lane >> 1) & 1) * 256 + (lane >> 2) * 16 + (lane & 1) * 8;
    unsigned o_pa = pa_data(kvh, w, b) + sp * 512 + lane * 8;
    unsigned a_cnt = LB + L_TAG + 4, one = 1u;
    PIN(vo); PIN(one); PIN(mk0); PIN(mk1); PIN(a_q); PIN(a_mw); PIN(a_mr); PIN(a_mo); PIN(a_ow); PIN(a_or); PIN(o_pa); PIN(a_cnt);
    unsigned a_k = LB + L_K + w * 8192 + (c & 3) * 1024 + (c >> 2) * 256 + ((g ^ c) * 16);
    PIN(a_k);
#pragma unroll
    for (int i = 0; i < NV0; ++i) vf[i] = LDV(i);
    RBAR();
#pragma unroll
    for (int i = NV0; i < NV1; ++i) vf[i] = LDV(i);
    unsigned goff = WS_QG + kvh * 4 * 8192 + b * 4096 + w * 1024 + lane * 16;
    unsigned a_qw = LB + L_QS + w * QS_PITCH + lane * 4;
    PIN(goff); PIN(a_qw);
    asm volatile("s_waitcnt vmcnt(%3) lgkmcnt(0)" : "+s"(pf0), "+s"(pf1), "+s"(pf2) : "i"(NV1) : "memory");
#pragma unroll
    for (int i = 0; i < 8; ++i) kf[i] = LD(v4u, (a_k ^ ((i & 3) * 64)) + (i >> 2) * 4096);
#pragma unroll
    for (int i = 0; i < 8; ++i) PIN(kf[i]);
    RBAR();
    v4u d[4];
#define QLOAD() _Pragma("unroll") for (int i = 0; i < 4; ++i) d[i] = __builtin_amdgcn_raw_buffer_load_b128(rs, goff, i * 8192, SC1)
    QLOAD();
    RBAR();
#pragma unroll
    for (int i = NV1; i < 8; ++i) vf[i] = LDV(i);
    tag = LD(unsigned, a_cnt - 4);
    PIN(tag);
    STAMP2(1);
    __builtin_amdgcn_s_setprio(2);
#define QBAD() ({ unsigned bad = 0; _Pragma("unroll") for (int i = 0; i < 4; ++i) bad = tagbad(bad, d[i], tag); ANY(bad >> 1); })
#define QPUT() do { LD(unsigned, a_qw) = pk_bf16((F_(d[0].x) + F_(d[1].x)) + (F_(d[2].x) + F_(d[3].x)), (F_(d[0].z) + F_(d[1].z)) + (F_(d[2].z) + F_(d[3].z))); \
    if (w == 0) asm volatile("s_mov_b64 exec, 1\n\tds_write_b32 %0, %1 offset:40\n\ts_mov_b64 exec, -1" ::"v"(a_cnt), "v"((d[0].y & d[1].y & d[2].y & d[3].y & 1u) << 13) : "memory"); } while (0)
    QPUT();
    if (__builtin_expect(QBAD(), 0)) {
#pragma unroll 1
      for (unsigned spin = 0; spin < SPIN_CAP && QBAD(); ++spin) {
        CBAR();
        QLOAD();
      }
      __builtin_amdgcn_s_waitcnt(0x0F70);
      QPUT();
    }
    BAR();
    STAMP(2);
    v4u qf[4];
#pragma unroll
    for (int s = 0; s < 4; ++s) qf[s] = LD(v4u, a_q + s * 64);
    npo = LD(unsigned, a_cnt + 40);
    __builtin_amdgcn_sched_barrier(0);
    f32x4 sc[2] = {};
#pragma unroll
    for (int s = 0; s < 4; ++s) { sc[0] = mfma(kf[s], qf[s], sc[0]); sc[1] = mfma(kf[4 + s], qf[s], sc[1]); __builtin_amdgcn_sched_barrier(0); }
    o_pa += npo;
    float sv[8];
#pragma unroll
    for (int i = 0; i < 4; ++i) {
      const unsigned r = pk_bf16(sc[i >> 1][(i & 1) * 2], sc[i >> 1][(i & 1) * 2 + 1]);
      const f32x2 t2 = f32x2{bf_lo(r), bf_hi(r)} * 0.08838834764831845f;
      const unsigned t = pk_bf16(t2[0], t2[1]);
      sv[2 * i] = bf_lo(t); sv[2 * i + 1] = bf_hi(t);
    }
    const float mw = xrows_max(vmax3(vmax3(sv[0], sv[1], sv[2]), vmax3(sv[3], sv[4], sv[5]), vmax(sv[6], sv[7]))) * LOG2E;
    asm volatile("s_mov_b64 exec, 0x1111\n\tds_write_b32 %0, %1\n\ts_mov_b64 exec, 1\n\tds_add_u32 %2, %3\n\ts_mov_b64 exec, -1"
                 ::"v"(a_mw), "v"(mw), "v"(a_cnt), "v"(one) : "memory");
    __builtin_amdgcn_sched_barrier(0);
    float e[8];
    f32x4 olo = {}, ohi = {};
    unsigned cnt2; f32x4 m0; float mo;
#pragma unroll
    for (int t = 0; t < 8; ++t) {
      e[t] = __builtin_amdgcn_exp2f(fmaf(sv[t], LOG2E, -mw));
      const unsigned pk = pk_bf16(e[t], e[t]);
      const v2u pa = v2u{pk & mk0, pk & mk1};
      olo = mfma16(pa, v2u{vf[t].x, vf[t].y}, olo); ohi = mfma16(pa, v2u{vf[t].z, vf[t].w}, ohi);
      if (t == 3) {
        __builtin_amdgcn_sched_barrier(0);
        cnt2 = *(volatile LAS unsigned*)(a_cnt); m0 = *(volatile LAS f32x4*)(a_mr); mo = *(volatile LAS float*)(a_mo);
        __builtin_amdgcn_sched_barrier(0);
      }
    }
    float fac = __builtin_amdgcn_exp2f(mo - vmax3(vmax(m0[0], m0[1]), m0[2], m0[3]));
    PIN(fac); PIN(olo); PIN(ohi);
    if (__builtin_expect(__builtin_amdgcn_readfirstlane(cnt2) < 4u, 0)) {
      do {
        cnt2 = *(volatile LAS unsigned*)(a_cnt);
        m0 = *(volatile LAS f32x4*)(a_mr);
      } while (__builtin_amdgcn_readfirstlane(cnt2) < 4u);
      fac = __builtin_amdgcn_exp2f(mo - vmax3(vmax(m0[0], m0[1]), m0[2], m0[3]));
    }
    STAMP2(10);
    LD(f32x4, a_ow) = olo * fac;
    LD(f32x4, a_ow + 256) = ohi * fac;
    asm volatile("s_mov_b64 exec, 1\n\tds_add_u32 %0, %1 offset:4\n\ts_mov_b64 exec, -1" ::"v"(a_cnt), "v"(one) : "memory");
    {
      const float lw = x32_sum(x16_sum(((e[0] + e[1]) + (e[2] + e[3])) + ((e[4] + e[5]) + (e[6] + e[7]))));
      asm volatile("s_mov_b64 exec, 0x1111\n\tds_write_b32 %0, %1 offset:128\n\ts_mov_b64 exec, 1\n\tds_add_u32 %2, %3 offset:12\n\ts_mov_b64 exec, -1"
                   ::"v"(a_mw), "v"(lw), "v"(a_cnt), "v"(one) : "memory");
    }
    STAMP2(4);
    {
      f32x2 r0, r1, r2, r3;
#pragma unroll 1
      for (;;) {
        const unsigned cnt = *(volatile LAS unsigned*)(a_cnt + 4);
        r0 = *(volatile LAS f32x2*)(a_or); r1 = *(volatile LAS f32x2*)(a_or + RED_PITCH);
        r2 = *(volatile LAS f32x2*)(a_or + 2 * RED_PITCH); r3 = *(volatile LAS f32x2*)(a_or + 3 * RED_PITCH);
        if (__builtin_amdgcn_readfirstlane(cnt) >= 4u) break;
      }
      const f32x2 a = (r0 + r1) + (r2 + r3);
      __builtin_amdgcn_raw_buffer_store_b64(v2u{pk_bf16(a[0], a[1]), tag}, rs, o_pa, 0, SC1);
    }
    asm volatile("s_mov_b64 exec, 1\n\tds_add_u32 %0, %1 offset:8\n\ts_mov_b64 exec, -1" ::"v"(a_cnt), "v"(one) : "memory");
    STAMP(5);
  }

  if (j < 16) {
    const int h = kvh * 4 + (j >> 2), cq = j & 3;
    if (wave == 0) {
      const unsigned mlo = pa_ml(kvh, j >> 2, g & 1) + c * 128 + npo;
      v2u dm, dl;
      asm volatile("buffer_load_dwordx2 %0, %2, %3, 0 offen sc1\n\tbuffer_load_dwordx2 %1, %2, %3, 0 offen offset:2048 sc1" : "=&v"(dm), "=&v"(dl) : "v"(mlo), "s"(rs) : "memory");
      asm volatile("s_waitcnt vmcnt(2)" ::: "memory");
      if (lane == 0) *(volatile LAS unsigned*)(LB + L_TAG + 20) = 1u;
      STAMP2(9);
      asm volatile("s_waitcnt vmcnt(0)" : "+v"(dm), "+v"(dl)::"memory");
#pragma unroll 1
      for (unsigned spin = 0; spin < SPIN_CAP && ANY(dm.y ^ tag); ++spin) {
        CBAR();
        dm = __builtin_amdgcn_raw_buffer_load_b64(rs, mlo, 0, SC1); dl = __builtin_amdgcn_raw_buffer_load_b64(rs, mlo + 2048, 0, SC1);
      }
      v2u dl2 = __builtin_amdgcn_raw_buffer_load_b64(rs, mlo + 2048, 0, SC1);
      const float m = F_(dm.x);
      float M = vmax(m, dpp<XOR1>(m)); M = vmax(M, dpp<XOR2>(M)); M = vmax(M, dpp<HMIR>(M)); M = vmax(M, dpp<ROR8>(M));
      const float ex = __builtin_amdgcn_exp2f(m - M);
      const unsigned wb = pk_bf16(ex, ex);
      if (g < 2) *(volatile LAS unsigned*)(LB + L_WN + g * 64 + (c & 3) * 16 + (c >> 2) * 4) = wb;
      if (lane == 0) *(volatile LAS unsigned*)(LB + L_TAG + 24) = 1u;
      STAMP2(8);
      if (ANY(dl.y ^ tag)) {
        dl = dl2;
#pragma unroll 1
        for (unsigned spin = 0; spin < SPIN_CAP && ANY(dl.y ^ tag); ++spin) { CBAR(); dl = __builtin_amdgcn_raw_buffer_load_b64(rs, mlo + 2048, 0, SC1); }
      }
      float S = bf_lo(wb) * F_(dl.x);
      S += dpp<XOR1>(S); S += dpp<XOR2>(S); S += dpp<HMIR>(S); S += dpp<ROR8>(S);
      if (g < 2 && c == 0) *(volatile LAS float*)(LB + L_TAG + 32 + g * 4) = __builtin_amdgcn_rcpf(S);
      if (lane == 0) *(volatile LAS unsigned*)(LB + L_TAG + 28) = 1u;
      STAMP(6);
      STAMP_FLUSH();
      return;
    }
    const int w = wave - 1;
    const int ob = w >> 1, dh = w & 1;
    unsigned off = pa_data(kvh, j >> 2, ob) + g * 512 + dh * 256 + c * 16 + npo;
    v4u d[4];
    unsigned a_ow = LB + L_ACT + ob * ACT_PITCH + dh * 128 + c * 8;
    unsigned a_or = LB + L_ACT + (c & 1) * ACT_PITCH + g * 16;
    unsigned o_pb = g > 1 ? 0x7ffffff0u : pb_inbox(cq * 8 + 2 * w + g) + h * 128 + c * 8;
    unsigned a_wn = LB + L_WN + ob * 64 + g * 16, a_fl = LB + L_TAG + 20, a_inv = LB + L_TAG + 32;
    const int q4 = (lane >> 2) & 3, pp = lane & 3;
    unsigned a_wf = LB + L_W + (8 * g + q4) * 256 + (((2 * w) ^ (q4 | ((g & 1) << 2))) * 32) + pp * 8;
    unsigned mk0 = (c & 2) ? 0u : ((c & 1) ? 0xffff0000u : 0x0000ffffu), mk1 = (c & 2) ? ((c & 1) ? 0xffff0000u : 0x0000ffffu) : 0u;
    unsigned one = 1u;
    PIN(off); PIN(a_ow); PIN(a_or); PIN(o_pb); PIN(a_wn); PIN(a_fl); PIN(a_wf); PIN(a_inv); PIN(mk0); PIN(mk1); PIN(one);
#define OLOAD() _Pragma("unroll") for (int i = 0; i < 4; ++i) d[i] = __builtin_amdgcn_raw_buffer_load_b128(rs, off + (i & 1) * 2048, (i >> 1) * 4096, SC1)
#define OOK() ({ unsigned bad = 0; _Pragma("unroll") for (int i = 0; i < 4; ++i) bad = tagbad(bad, d[i], tag); !ANY(bad); })
    OLOAD();
    while (__builtin_amdgcn_readfirstlane(*(volatile LAS unsigned*)(a_fl)) == 0u) {}
    CBAR();
    v4u wf[8];
#pragma unroll
    for (int s = 0; s < 8; ++s) wf[s] = tr_frag((a_wf ^ ((s & 1) * 32)) + (s >> 1) * 8192, (a_wf ^ ((s & 1) * 32)) + (s >> 1) * 8192 + 1024);
    v4u wn; unsigned fl;
#pragma unroll 1
    for (unsigned spin = 0; spin < SPIN_CAP; ++spin) {
      fl = *(volatile LAS unsigned*)(a_fl + 4);
      wn = *(volatile LAS v4u*)(a_wn);
      if (OOK()) break;
      CBAR(); OLOAD();
    }
    while (__builtin_amdgcn_readfirstlane(fl) == 0u) {
      fl = *(volatile LAS unsigned*)(a_fl + 4);
      wn = *(volatile LAS v4u*)(a_wn);
    }
    v2u wa[4];
#pragma unroll
    for (int i = 0; i < 4; ++i) wa[i] = v2u{wn[i] & mk0, wn[i] & mk1};
    STAMP(6);
    {
      f32x4 o = {};
#pragma unroll
      for (int i = 0; i < 4; ++i) o = mfma16(wa[i], v2u{d[i].x, d[i].z}, o);
      if (g == 0) LD(v2u, a_ow) = v2u{pk_bf16(o[0], o[1]), pk_bf16(o[2], o[3])};
    }
    asm volatile("s_mov_b64 exec, 1\n\tds_add_u32 %0, %1 offset:28\n\ts_mov_b64 exec, -1" ::"v"(a_fl), "v"(one) : "memory");
    f32x4 acc0 = {}, acc1 = {};
    v4u of[4];
#pragma unroll 1
    for (;;) {
      const unsigned cnt = *(volatile LAS unsigned*)(a_fl + 28);
#pragma unroll
      for (int s = 0; s < 4; ++s) of[s] = *(volatile LAS v4u*)(a_or + s * 64);
      if (__builtin_amdgcn_readfirstlane(cnt) >= 4u) break;
    }
    unsigned fi = *(volatile LAS unsigned*)(a_fl + 8);
    f32x2 inv = *(volatile LAS f32x2*)(a_inv);
    __builtin_amdgcn_sched_barrier(0);
#pragma unroll
    for (int s = 0; s < 4; ++s) { acc0 = mfma(of[s], wf[2 * s], acc0); acc1 = mfma(of[s], wf[2 * s + 1], acc1); }
    float y0 = g ? acc1[0] : acc0[0], y1 = g ? acc1[1] : acc0[1];
    PIN(y0); PIN(y1);
    while (__builtin_expect(__builtin_amdgcn_readfirstlane(fi) == 0u, 0)) { fi = *(volatile LAS unsigned*)(a_fl + 8); inv = *(volatile LAS f32x2*)(a_inv); }
    __builtin_amdgcn_raw_buffer_store_b64(v2u{pk_bf16(y0 * inv[0], y1 * inv[1]), tag}, rs, o_pb, 0, SC1);
    STAMP(7);
  } else if (j < 23) {
    const int f = kvh * 7 + (j - 16);
    if (wave == 0) {
      asm volatile("s_waitcnt vmcnt(0)" ::: "memory");
      STAMP(6);
      RBAR();
      RBAR();
      unsigned a_h = LB + L_H + ((lane >> 2) & 1) * H_PITCH + (lane >> 3) * 32 + (lane & 3) * 8;
      unsigned a_st = LB + L_WN + ((lane >> 2) & 1) * 8;
      PIN(a_h); PIN(a_st);
      v4u d[4];
      poll<4, 1024>(rs, WS_HG + (kvh & 1) * 16384 + npo + ((kvh >> 1) & 1) * 4096 + lane * 16, tag, d);
      STAMP(7);
#pragma unroll
      for (int i = 0; i < 4; ++i) LD(v2u, a_h + i * 256) = v2u{d[i].x, d[i].z};
      BAR();
      PIN(d[0]); PIN(d[1]); PIN(d[2]); PIN(d[3]);
      float s0 = 0.f, s1 = 0.f, q0 = 0.f, q1 = 0.f;
#pragma unroll
      for (int i = 0; i < 4; ++i) {
        s0 = dot2(d[i].x, 0x3f803f80u, s0); s1 = dot2(d[i].z, 0x3f803f80u, s1);
        q0 = dot2(d[i].x, d[i].x, q0); q1 = dot2(d[i].z, d[i].z, q1);
      }
      float t = x16_sum2(s0 + s1, q0 + q1);
      t += dpp<XOR1>(t); t += dpp<XOR2>(t); t += dpp<ROR8>(t);
      t = x32_sum(t) * (1.0f / 512.0f);
      auto u = __builtin_amdgcn_permlane16_swap(U_(t), U_(t), false, false);
      const float mean = F_(u[0]);
      const float var = fmaxf(fmaf(-mean, mean, F_(u[1])), 0.f);
      const float inv = __builtin_amdgcn_rcpf(rbf(__builtin_amdgcn_sqrtf(rbf(rbf(var) + 1e-5f))));
      LD(f32x2, a_st) = f32x2{rbf(mean), inv};
      BAR();
      STAMP(8);
      STAMP_FLUSH();
      return;
    }
    const int w = wave - 1;
    float bias1 = bf_lo(p.b1[f * 32 + (lane & 31)]);
    RBAR();
    v4u w1f[8], w2f[8];
    {
      const int q4 = (lane >> 2) & 3, pp = lane & 3;
      const unsigned b1a = LB + L_W + (128 * w + 8 * g + q4) * 64 + ((g & 1) * 32) + pp * 8;
#pragma unroll
      for (int s = 0; s < 8; ++s) w1f[s] = tr_frag((b1a ^ ((s & 1) * 32)) + (s >> 1) * 2048, (b1a ^ ((s & 1) * 32)) + (s >> 1) * 2048 + 256);
      const unsigned b2a = LB + L_W + 32768 + (8 * g + q4) * 1024 + w * 256 + ((q4 | ((g & 1) << 2)) * 32) + pp * 8;
#pragma unroll
      for (int t = 0; t < 8; ++t) w2f[t] = tr_frag(b2a ^ (t * 32), (b2a ^ (t * 32)) + 4096);
    }
    const f32x4 z4 = {};
    unsigned a_zw = LB + L_ZP + ((g & 1) * 32 + (g >> 1) * 16 + c) * 16 + w * 4;
    unsigned a_zr = LB + L_ZP + lane * 16;
    unsigned a_ha = LB + L_H + (c & 1) * H_PITCH + w * 256 + g * 16;
    unsigned a_st = LB + L_WN + (lane >> 5) * 8;
    unsigned a_gw = LB + L_GA + w * 160 + (lane >> 5) * 80 + (lane & 31) * 2, a_gr = LB + L_GA + w * 160 + (c & 1) * 80 + g * 16;
    unsigned o_pd = WS_PD + (8 * w + g) * 8192 + f * 128 + c * 8;
    float csum;
    {
      const v4u ones = {0x3f803f80u, 0x3f803f80u, 0x3f803f80u, 0x3f803f80u};
      f32x4 c0 = z4, c1 = z4;
#pragma unroll
      for (int s = 0; s < 4; ++s) { c0 = mfma(ones, w1f[2 * s], c0); c1 = mfma(ones, w1f[2 * s + 1], c1); }
      LD(float, a_zw) = (g & 2) ? c1[0] : c0[0];
      BAR();
      const f32x4 a = LD(f32x4, a_zr);
      csum = (a[0] + a[1]) + (a[2] + a[3]);
      asm volatile("" : "+v"(csum), "+v"(w2f[0]), "+v"(w2f[1]), "+v"(w2f[2]), "+v"(w2f[3]), "+v"(w2f[4]), "+v"(w2f[5]), "+v"(w2f[6]), "+v"(w2f[7]), "+v"(bias1));
      PIN(a_zw); PIN(a_zr); PIN(a_ha); PIN(a_st); PIN(a_gw); PIN(a_gr); PIN(o_pd);
    }
    BAR();
    STAMP2(7);
    {
      v4u ha[4];
#pragma unroll
      for (int s = 0; s < 4; ++s) ha[s] = LD(v4u, a_ha + s * 64);
      __builtin_amdgcn_sched_barrier(0);
      f32x4 z0 = z4, z1 = z4;
#pragma unroll
      for (int s = 0; s < 4; ++s) { z0 = mfma(ha[s], w1f[2 * s], z0); z1 = mfma(ha[s], w1f[2 * s + 1], z1); }
      const float t0 = (g & 1) ? z0[1] : z0[0], t1 = (g & 1) ? z1[1] : z1[0];
      LD(float, a_zw) = (g & 2) ? t1 : t0;
    }
    BAR();
    {
      const f32x4 a = LD(f32x4, a_zr);
      const f32x2 st = LD(f32x2, a_st);
      const float zr = (a[0] + a[1]) + (a[2] + a[3]);
      const float z = rbf(rbf(fmaf(-st[0], csum, zr) * st[1]) + bias1);
      const float t = fmaf(z * z, 0.044715f * 0.7978845608028654f * 2.f * LOG2E, 0.7978845608028654f * 2.f * LOG2E);
      const float e = __builtin_amdgcn_exp2f(z * t);
      const float gl = fmaf(-z, __builtin_amdgcn_rcpf(e + 1.0f), z);
      LD(u16, a_gw) = (u16)pk_bf16(gl, gl);
    }
    CBAR();
    const v4u ga = LD(v4u, a_gr);
#pragma unroll
    for (int u = 0; u < 2; ++u) {
      const f32x4 y0 = mfma(ga, w2f[4 * u], z4), y1 = mfma(ga, w2f[4 * u + 1], z4), y2 = mfma(ga, w2f[4 * u + 2], z4), y3 = mfma(ga, w2f[4 * u + 3], z4);
      const unsigned p0 = pk_bf16(y0[0], y0[1]), p1 = pk_bf16(y1[0], y1[1]), p2 = pk_bf16(y2[0], y2[1]), p3 = pk_bf16(y3[0], y3[1]);
      __builtin_amdgcn_raw_buffer_store_b64(v2u{(g & 2) ? ((g & 1) ? p3 : p2) : ((g & 1) ? p1 : p0), tag}, rs, o_pd, u * 4 * 8192, SC1);
    }
    STAMP(8);
  } else if (j < 31 && wave == 0) {
    const unsigned np = npo >> 13;
    const unsigned role = claim_role(p.ws, (((j - 23) * 4 + (kvh & 3)) * 2) | np, tag);
    const int r = role & 31, n = lane & 7, pg = lane >> 3;
    if (j < 27) {
      unsigned xr = *(const unsigned*)(p.x + (g & 1) * 512 + r * 16 + 2 * n);
      unsigned o_hg = WS_HG + ((lane >> 3) & 1) * 8192 + (g >> 1) * 4096 + r * 128 + ((g & 1) * 8 + n) * 8;
      PIN(xr); PIN(o_hg);
      STAMP2(9);
      const f32x4 a = poll_sum<4>(rs, pb_inbox(r) + lane * 16, tag);
      STAMP(6);
      float t = x32_sum2(x16_sum2(a[0], a[1]), x16_sum2(a[2], a[3]));
      t += dpp<ROR8>(t);
      auto sw = __builtin_amdgcn_permlane32_swap(U_(t), U_(t), false, false);
      const unsigned ar = pk_bf16(F_(sw[0]), F_(sw[1]));
      const unsigned pk = pk_bf16(bf_lo(xr) + bf_lo(ar), bf_hi(xr) + bf_hi(ar));
      __builtin_amdgcn_raw_buffer_store_b64(v2u{pk, tag}, rs, o_hg, 0, SC1);
      __builtin_amdgcn_raw_buffer_store_b64(v2u{pk, tag}, rs, o_hg, 16384, SC1);
      __builtin_amdgcn_raw_buffer_store_b64(v2u{pk, tag}, rs, o_hg, 32768, SC1);
      if (r == 0 && lane == 0) __builtin_amdgcn_raw_buffer_store_b32(tag, rs, WS_EPOCH, 0, SC1);
      STAMP(7);
    } else {
      unsigned b2r = *(const unsigned*)(p.b2 + r * 16 + 2 * n);
      v2u hg;
      const unsigned hoff = WS_HG + 32768 + np * 8192 + r * 128 + ((g & 1) * 8 + n) * 8;
#pragma unroll 1
      for (unsigned spin = 0; spin < SPIN_CAP; ++spin) {
        CBAR();
        hg = __builtin_amdgcn_raw_buffer_load_b64(rs, hoff, 0, SC1);
        if (__all(hg.y == tag)) break;
        __builtin_amdgcn_s_sleep(2);
      }
      unsigned o_out = ((g & 1) * 512 + r * 16 + 2 * n) * 2;
      PIN(b2r); PIN(o_out);
      const f32x4 a = poll_sum<7>(rs, WS_PD + r * 8192 + lane * 16, tag);
      STAMP(8);
      float t = x32_sum2(x16_sum2(a[0], a[1]), x16_sum2(a[2], a[3]));
      t += dpp<ROR8>(t);
      auto sw = __builtin_amdgcn_permlane32_swap(U_(t), U_(t), false, false);
      const unsigned ar = pk_bf16(F_(sw[0]), F_(sw[1]));
      const unsigned yr = pk_bf16(bf_lo(ar) + bf_lo(b2r), bf_hi(ar) + bf_hi(b2r));
      const unsigned pk = pk_bf16(bf_lo(hg.x) + bf_lo(yr), bf_hi(hg.x) + bf_hi(yr));
      if ((lane & 40) == 0) *(unsigned*)((unsigned char*)p.out + o_out) = pk;
      STAMP(9);
    }
  }
  STAMP_FLUSH();
}

}

extern "C" void kernel_launch(void* const* d_in, const int* in_sizes, int n_in,
                              void* d_out, int out_size, void* d_ws, size_t ws_size,
                              hipStream_t stream) {
  Params p;
  p.x = (const u16*)d_in[0]; p.kc = (const u16*)d_in[1]; p.vc = (const u16*)d_in[2];
  p.wq = (const u16*)d_in[3]; p.wo = (const u16*)d_in[4];
  p.w1 = (const u16*)d_in[6]; p.b1 = (const u16*)d_in[7]; p.w2 = (const u16*)d_in[8]; p.b2 = (const u16*)d_in[9];
  p.out = (u16*)d_out; p.ws = (unsigned char*)(((uintptr_t)d_ws + 16383) & ~(uintptr_t)16383);
  (void)hipFuncSetAttribute((const void*)fused_decode, hipFuncAttributeMaxDynamicSharedMemorySize, LDS_BYTES);
  hipLaunchKernelGGL(fused_decode, dim3(256), dim3(NTHR), LDS_BYTES, stream, p);
}
```

```cpp
#include <hip/hip_runtime.h>
#include <hip/hip_bf16.h>
#include <stdint.h>

typedef unsigned short u16;
typedef unsigned long long u64;
typedef unsigned v4u __attribute__((ext_vector_type(4)));
typedef unsigned v2u __attribute__((ext_vector_type(2)));
typedef float f32x4 __attribute__((ext_vector_type(4)));
typedef float f32x2 __attribute__((ext_vector_type(2)));
typedef short bf16x8 __attribute__((ext_vector_type(8)));
typedef short bf16x4 __attribute__((ext_vector_type(4)));
typedef __bf16 bf16x2 __attribute__((ext_vector_type(2)));
typedef short v4i16 __attribute__((ext_vector_type(4)));
#define LAS __attribute__((address_space(3)))
#define GAS __attribute__((address_space(1)))

namespace {

constexpr int SK = 8192, WIN = 2048, FF = 1792;
constexpr int NTHR = 320;
#ifndef NV0
#define NV0 1
#endif
#ifndef NV1
#define NV1 5
#endif
#define WN1F 16
#define TLOOK 5400
#define CODE_BYTES 12480
constexpr unsigned SPIN_CAP = 1u << 18;
constexpr float LOG2E = 1.4426950408889634f;

constexpr unsigned WS_EPOCH = 0;
constexpr unsigned WS_CLAIM = 1024;
constexpr unsigned WS_CAL = 16384;
constexpr unsigned WS_HG = WS_CAL + 65536;
constexpr unsigned WS_PB = WS_HG + 3 * 16384;
constexpr unsigned WS_PD = WS_PB + 8 * 16384;
constexpr unsigned WS_PA = WS_PD + 32 * 8192;
constexpr unsigned WS_QG = WS_PA + 8 * 9 * 16384;
constexpr unsigned WS_END = WS_QG + 8 * 4 * 8192;
__host__ __device__ constexpr unsigned pa_data(unsigned kvh, unsigned hh, unsigned b) { return WS_PA + (kvh * 9 + hh * 2 + b) * 16384; }
__host__ __device__ constexpr unsigned pa_ml(unsigned kvh, unsigned hh, unsigned b) { return WS_PA + (kvh * 9 + 8) * 16384 + b * 4096 + hh * 8; }
constexpr unsigned WS_STAMP = 4u << 20;
__host__ __device__ constexpr unsigned pb_inbox(unsigned r) { return WS_PB + (r >> 2) * 16384 + (r & 1) * 8192 + ((r >> 1) & 1) * 4096; }

constexpr int L_WQ = 0;
constexpr int RED_PITCH = 2048;
constexpr int L_RED = 0;
constexpr int L_W = 17408;
constexpr int L_K = L_W + 32768;
constexpr int L_ACT = L_W + 65536;
constexpr int ACT_PITCH = 256 + 64;
constexpr int L_QS = L_ACT + 2 * ACT_PITCH;
constexpr int QS_PITCH = 256 + 64;
constexpr int L_MX = L_QS + 4 * QS_PITCH;
constexpr int L_LS = L_MX + 128;
constexpr int L_WN = L_LS + 128;
constexpr int L_ZP = L_WN + 128;
constexpr int L_GA = L_ZP + 64 * 48;
constexpr int L_H = L_GA + 8 * 160;
constexpr int H_PITCH = 1024 + 64;
constexpr int L_TAG = L_H + 2 * H_PITCH;
constexpr int LDS_BYTES = L_TAG + 64;
static_assert(8 * RED_PITCH <= L_W, "lds map");
static_assert(LDS_BYTES > 81920, "one workgroup per CU");

struct Params {
  const u16* x; const u16* kc; const u16* vc; const u16* wq; const u16* wo;
  const u16* w1; const u16* b1; const u16* w2; const u16* b2;
  u16* out; unsigned char* ws;
};

#define F_(v) __uint_as_float(v)
#define U_(v) __float_as_uint(v)
__device__ __forceinline__ float bf_lo(unsigned d) { return F_(d << 16); }
__device__ __forceinline__ float bf_hi(unsigned d) { return F_(d & 0xffff0000u); }
__device__ __forceinline__ unsigned pk_bf16(float lo, float hi) {
  return __builtin_bit_cast(unsigned, __builtin_convertvector(f32x2{lo, hi}, bf16x2));
}
__device__ __forceinline__ float rbf(float f) { return bf_lo(pk_bf16(f, f)); }
__device__ __forceinline__ f32x4 mfma(v4u a, v4u b, f32x4 c) {
  return __builtin_amdgcn_mfma_f32_16x16x32_bf16(__builtin_bit_cast(bf16x8, a), __builtin_bit_cast(bf16x8, b), c, 0, 0, 0);
}
__device__ __forceinline__ f32x4 mfma16(v2u a, v2u b, f32x4 c) {
  return __builtin_amdgcn_mfma_f32_16x16x16bf16_1k(__builtin_bit_cast(bf16x4, a), __builtin_bit_cast(bf16x4, b), c, 0, 0, 0);
}
__device__ __forceinline__ float dot2(unsigned a, unsigned b, float acc) {
  return __builtin_amdgcn_fdot2_f32_bf16(__builtin_bit_cast(bf16x2, a), __builtin_bit_cast(bf16x2, b), acc, false);
}
template <int CTRL> __device__ __forceinline__ float dpp(float x) {
  return __builtin_bit_cast(float, __builtin_amdgcn_mov_dpp(__builtin_bit_cast(int, x), CTRL, 0xf, 0xf, true));
}
constexpr int XOR1 = 0xB1, XOR2 = 0x4E, HMIR = 0x141, ROR8 = 0x128;
__device__ __forceinline__ float x16_sum(float x) {
  auto s = __builtin_amdgcn_permlane16_swap(U_(x), U_(x), false, false);
  return F_(s[0]) + F_(s[1]);
}
__device__ __forceinline__ float x32_sum(float x) {
  auto s = __builtin_amdgcn_permlane32_swap(U_(x), U_(x), false, false);
  return F_(s[0]) + F_(s[1]);
}
__device__ __forceinline__ float x16_sum2(float a, float b) {
  auto s = __builtin_amdgcn_permlane16_swap(U_(a), U_(b), false, false);
  return F_(s[0]) + F_(s[1]);
}
__device__ __forceinline__ float x32_sum2(float a, float b) {
  auto s = __builtin_amdgcn_permlane32_swap(U_(a), U_(b), false, false);
  return F_(s[0]) + F_(s[1]);
}
__device__ __forceinline__ float vmax(float a, float b) { float r; asm("v_max_f32 %0, %1, %2" : "=v"(r) : "v"(a), "v"(b)); return r; }
__device__ __forceinline__ float vmax3(float a, float b, float c) { float r; asm("v_max3_f32 %0, %1, %2, %3" : "=v"(r) : "v"(a), "v"(b), "v"(c)); return r; }
__device__ __forceinline__ float xrows_max(float x) {
  auto s = __builtin_amdgcn_permlane16_swap(U_(x), U_(x), false, false);
  x = vmax(F_(s[0]), F_(s[1]));
  auto t = __builtin_amdgcn_permlane32_swap(U_(x), U_(x), false, false);
  return vmax(F_(t[0]), F_(t[1]));
}
#define BAR() asm volatile("s_waitcnt lgkmcnt(0)\n\ts_barrier" ::: "memory")
#define RBAR() asm volatile("s_barrier" ::: "memory")
#define CBAR() asm volatile("" ::: "memory")
#define PIN(x) asm volatile("" : "+v"(x))
#define LDNT(p) __builtin_nontemporal_load((const v4u*)(p))
#define SC1 16
#define LD(T, a) (*(LAS T*)(a))

__device__ __forceinline__ void dma16(const void* sbase, unsigned voff, unsigned ldsaddr) {
  asm volatile("s_mov_b32 m0, %2\n\ts_nop 0\n\tglobal_load_lds_dwordx4 %0, %1 nt" ::"v"(voff), "s"(sbase), "s"(ldsaddr) : "memory");
}
__device__ __forceinline__ v4u tr_frag(unsigned a0, unsigned a1) {
  v2u a = __builtin_bit_cast(v2u, __builtin_amdgcn_ds_read_tr16_b64_v4i16((LAS v4i16*)a0));
  v2u b = __builtin_bit_cast(v2u, __builtin_amdgcn_ds_read_tr16_b64_v4i16((LAS v4i16*)a1));
  return v4u{a.x, a.y, b.x, b.y};
}
__device__ __forceinline__ v4u ldg(__amdgpu_buffer_rsrc_t rs, unsigned off) { return __builtin_amdgcn_raw_buffer_load_b128(rs, off, 0, SC1); }

__device__ __forceinline__ void cal_issue(const unsigned char* ws) {
  const unsigned o = WS_CAL + (blockIdx.x >> 6) * 16384 + (blockIdx.x & 63) * 128;
  asm volatile("s_mov_b64 exec, 1\n\tglobal_store_dword %0, %0, %2 sc1\n\tglobal_store_dword %1, %0, %2 sc1\n\ts_mov_b64 exec, -1"
               ::"v"(o), "v"(o + 8192), "s"(ws) : "memory");
}
__device__ __forceinline__ unsigned cal_result() {
  u64 t1, t2;
  asm volatile("s_waitcnt vmcnt(1)\n\ts_memtime %0\n\ts_waitcnt vmcnt(0)\n\ts_memtime %1\n\ts_waitcnt lgkmcnt(0)" : "=&s"(t1), "=&s"(t2)::"memory");
  return ((unsigned)t2 - (unsigned)t1) > 100u ? 0u : 1u;
}
__device__ __forceinline__ unsigned claim_role(unsigned char* ws, unsigned role, unsigned tag) {
  unsigned old = 0;
  if ((threadIdx.x & 63) == 0) old = __hip_atomic_exchange((GAS unsigned*)(ws + WS_CLAIM + role * 128), tag, __ATOMIC_RELAXED, __HIP_MEMORY_SCOPE_AGENT);
  return __builtin_amdgcn_readfirstlane(old) == tag ? role ^ 1u : role;
}

__device__ __forceinline__ unsigned tagbad(unsigned bad, v4u d, unsigned tag) {
  return __builtin_amdgcn_bitop3_b32(__builtin_amdgcn_bitop3_b32(bad, d.y, tag, 0xF6), d.w, tag, 0xF6);
}
#define ANY(bad) (__builtin_amdgcn_ballot_w64((bad) != 0u) != 0ull)
template <int N, int STRIDE>
__device__ __forceinline__ void poll(__amdgpu_buffer_rsrc_t rs, unsigned off, unsigned tag, v4u (&d)[N]) {
  unsigned n = SPIN_CAP;
#pragma unroll 1
  for (;;) {
    asm volatile("" : "+s"(n)::"memory");
#pragma unroll
    for (int i = 0; i < N; ++i) d[i] = __builtin_amdgcn_raw_buffer_load_b128(rs, off + (i & 3) * STRIDE, (i >> 2) * 4 * STRIDE, SC1);
    unsigned bad = 0;
#pragma unroll
    for (int i = 0; i < N; ++i) bad = tagbad(bad, d[i], tag);
    if (!ANY(bad)) break;
    if (--n == 0u) break;
  }
}

template <int N>
__device__ __forceinline__ f32x4 poll_sum(__amdgpu_buffer_rsrc_t rs, unsigned off, unsigned tag) {
  unsigned n = SPIN_CAP, klo = 0x00003f80u, khi = 0x3f800000u;
  PIN(klo); PIN(khi);
  f32x4 s;
#pragma unroll 1
  for (;;) {
    asm volatile("" : "+s"(n)::"memory");
    v4u d[N];
#pragma unroll
    for (int i = 0; i < N; ++i) d[i] = __builtin_amdgcn_raw_buffer_load_b128(rs, off + (i & 3) * 1024, (i >> 2) * 4096, SC1);
    unsigned bad = 0;
    s = f32x4{0.f, 0.f, 0.f, 0.f};
#pragma unroll
    for (int i = 0; i < N; ++i) {
      s[0] = dot2(d[i].x, klo, s[0]); s[1] = dot2(d[i].x, khi, s[1]);
      s[2] = dot2(d[i].z, klo, s[2]); s[3] = dot2(d[i].z, khi, s[3]);
      bad = tagbad(bad, d[i], tag);
    }
    PIN(s);
    if (!ANY(bad)) break;
    if (--n == 0u) break;
  }
  return s;
}
__device__ __forceinline__ float half_sum(float x) {
  x += dpp<XOR1>(x); x += dpp<XOR2>(x); x += dpp<HMIR>(x); x += dpp<ROR8>(x);
  return x16_sum(x);
}
__device__ __forceinline__ void ln_stats(v4u a, v4u b, float& mb, float& inv) {
  float s0 = 0.f, s1 = 0.f;
#pragma unroll
  for (int i = 0; i < 4; ++i) { s0 = dot2(a[i], 0x3f803f80u, s0); s1 = dot2(b[i], 0x3f803f80u, s1); }
  const float mean = half_sum(s0 + s1) * (1.0f / 512.0f);
  f32x2 q0 = {0.f, 0.f}, q1 = {0.f, 0.f};
#pragma unroll
  for (int i = 0; i < 4; ++i) {
    const f32x2 u = f32x2{bf_lo(a[i]), bf_hi(a[i])} - mean, v = f32x2{bf_lo(b[i]), bf_hi(b[i])} - mean;
    q0 = u * u + q0; q1 = v * v + q1;
  }
  q0 += q1;
  const float var = half_sum(q0[0] + q0[1]) * (1.0f / 512.0f);
  mb = rbf(mean);
  inv = __builtin_amdgcn_rcpf(rbf(__builtin_amdgcn_sqrtf(rbf(rbf(var) + 1e-5f))));
}

#define STAMP(i) do {} while (0)
#define STAMP_DECL
#define STAMP_FLUSH() do {} while (0)
#define STAMP2(i) do {} while (0)

__global__ void __launch_bounds__(NTHR, 1) __attribute__((aligned(4096))) fused_decode(Params p) {
  extern __shared__ __attribute__((aligned(16))) unsigned char lds_raw[];
  u64 pc;
  asm volatile("s_getpc_b64 %0" : "=s"(pc));
  const unsigned LB = (unsigned)(size_t)(LAS unsigned char*)lds_raw;
  const int tid = threadIdx.x, lane = tid & 63, hwave = __builtin_amdgcn_readfirstlane(tid >> 6);
  const int wave = hwave == 4 ? 0 : hwave + 1;
  const int kvh = blockIdx.x & 7, j = blockIdx.x >> 3;
  const int g = lane >> 4, c = lane & 15;
  STAMP_DECL;
  STAMP(0);
  unsigned t_start = 0;
  const __amdgpu_buffer_rsrc_t rs = __builtin_amdgcn_make_buffer_rsrc((void*)p.ws, 0, WS_END, 0x00020000);
  const int b = j >> 4, sp = j & 15;
  unsigned tag, npo;

  const int rg = j >> 3, cg = j & 7;
  const int dph = j + kvh * 5;
  unsigned ep; v4u x0, x1; v2u xs;
  if (wave > 1) {
    unsigned o = (j * 3 + wave - 2) * 192, t0, t1, t2;
    o = o < CODE_BYTES - 192 ? o : CODE_BYTES - 192;
    asm volatile("s_load_dword %0, %3, 0x0\n\ts_load_dword %1, %3, 0x40\n\ts_load_dword %2, %3, 0x80\n\ts_waitcnt lgkmcnt(0)"
                 : "=&s"(t0), "=&s"(t1), "=&s"(t2) : "s"((pc & ~255ull) + o));
  }
  asm volatile("" ::"s"(p.ws), "s"(p.kc), "s"(p.out));
  if (wave == 1) { cal_issue(p.ws); const unsigned np0 = cal_result(); if (lane == 0) LD(unsigned, LB + L_TAG + 40) = np0; asm volatile("s_waitcnt lgkmcnt(0)" ::: "memory"); }
  if (wave == 0) {
    __builtin_amdgcn_s_setprio(3);
    asm volatile("global_load_dword %0, %1, %2 sc1" : "=v"(ep) : "v"(WS_EPOCH), "s"(p.ws) : "memory");
    asm volatile("global_load_dwordx4 %0, %1, %2" : "=v"(x0) : "v"(lane * 32), "s"(p.x) : "memory");
    asm volatile("global_load_dwordx4 %0, %1, %2 offset:16" : "=v"(x1) : "v"(lane * 32), "s"(p.x) : "memory");
    asm volatile("global_load_dwordx2 %0, %1, %2" : "=v"(xs) : "v"((lane >> 5) * 1024 + rg * 256 + (lane & 31) * 8), "s"(p.x) : "memory");
    { u64 t; asm volatile("s_memtime %0\n\ts_waitcnt lgkmcnt(0)" : "=s"(t)); t_start = (unsigned)t; }
    STAMP2(10);
    const int r0 = lane >> 3, pos = lane & 7;
    const unsigned v0 = r0 * 8192 + (((pos >> 1) ^ ((r0 >> 1) & 1)) * 32) + (pos & 1) * 16;
    const u16* src = p.wq + (size_t)(rg * 128) * 4096 + kvh * 512 + cg * 64;
    const unsigned v1 = (v0 ^ 64u) + 65536u, va = (dph & 8) ? v1 : v0, vb = (dph & 8) ? v0 : v1, la = (dph & 8) ? 1024u : 0u;
#pragma unroll 1
    for (int i = 0; i < 8; ++i) {
      const int ii = i ^ (dph & 7);
      dma16(src + (size_t)ii * 16 * 4096, va, LB + L_WQ + ii * 2048 + la);
      dma16(src + (size_t)ii * 16 * 4096, vb, LB + L_WQ + ii * 2048 + (la ^ 1024));
    }
  }
  const size_t kvbase = ((size_t)(b * 8 + kvh) * SK + (SK - WIN) + sp * 128) * 128;
  const unsigned char* kp = (const unsigned char*)(p.kc + kvbase);
  const unsigned char* vp = (const unsigned char*)(p.vc + kvbase);
  const int pk0 = (((wave - 1) * 8) ^ dph) & 31;
  unsigned ko = g * 256 + ((c ^ (4 * g)) * 16);
  PIN(ko);
  RBAR();
  {
    int wave1 = wave;
    asm volatile("" : "+s"(wave1));
    if (wave1 != 0) {
      const int w = wave - 1;
#define DMAK(i) dma16(kp + (pk0 ^ (i)) * 1024, ko ^ (((i) & 3) * 16), LB + L_K + w * 8192 + (i) * 1024)
#pragma unroll
      for (int i = 0; i < 8; ++i) DMAK(i);
    }
  }
  int wave2 = wave;
  asm volatile("" : "+s"(wave2));

  if (__builtin_expect(wave2 == 0, 1)) {
    if (lane < 8) LD(unsigned, LB + L_TAG + 4 + lane * 4) = 0u;
    if (lane == 8) LD(unsigned, LB + L_TAG + 48) = 0u;
    const int q4 = (lane >> 2) & 3, pp = lane & 3;
    unsigned tra = LB + L_WQ + (8 * g + q4) * 128 + pp * 8 + ((((q4 >> 1) & 1) | ((g & 1) << 1)) * 32) + ((dph >> 1) & 3) * 4096;
    unsigned qoff = WS_QG + (kvh * 4 + rg) * 8192 + (cg * 64 + lane) * 8;
    unsigned a_ln = LB + L_ACT + (lane >> 5) * ACT_PITCH + (lane & 31) * 8;
    unsigned a_af = LB + L_ACT + (c & 1) * ACT_PITCH + ((dph >> 1) & 3) * 64;
    unsigned af0 = a_af + g * 16, af1 = (((dph >> 1) & 3) ^ 1) * 64 + LB + L_ACT + (c & 1) * ACT_PITCH + g * 16;
    unsigned af2 = (((dph >> 1) & 3) ^ 2) * 64 + LB + L_ACT + (c & 1) * ACT_PITCH + g * 16, af3 = (((dph >> 1) & 3) ^ 3) * 64 + LB + L_ACT + (c & 1) * ACT_PITCH + g * 16;
    unsigned trs[16];
#pragma unroll
    for (int i = 0; i < 16; ++i) { trs[i] = tra ^ ((i & 3) * 32 + (i >> 2) * 4096); PIN(trs[i]); }
    PIN(qoff); PIN(a_ln); PIN(af0); PIN(af1); PIN(af2); PIN(af3);
    asm volatile("s_waitcnt vmcnt(16)" : "+v"(ep), "+v"(x0), "+v"(x1), "+v"(xs)::"memory");
    STAMP2(1);
    v4u af[4];
    {
      float mb, inv;
      ln_stats(x0, x1, mb, inv);
      const unsigned c0 = pk_bf16(bf_lo(xs.x) - mb, bf_hi(xs.x) - mb), c1 = pk_bf16(bf_lo(xs.y) - mb, bf_hi(xs.y) - mb);
      LD(v2u, a_ln) = v2u{pk_bf16(bf_lo(c0) * inv, bf_hi(c0) * inv), pk_bf16(bf_lo(c1) * inv, bf_hi(c1) * inv)};
      CBAR();
      af[0] = LD(v4u, af0); af[1] = LD(v4u, af1); af[2] = LD(v4u, af2); af[3] = LD(v4u, af3);
      CBAR();
    }
    tag = (__builtin_amdgcn_readfirstlane(ep) | 1u) + 1u;
    if (lane == 0) LD(unsigned, LB + L_TAG) = tag;
    const unsigned np0 = LD(unsigned, LB + L_TAG + 40);
    STAMP2(5);
    f32x4 acc[4] = {};
#pragma unroll
    for (int s = 0; s < 4; ++s) {
      if (s == 0) asm volatile("s_waitcnt vmcnt(12)" ::: "memory");
      if (s == 1) asm volatile("s_waitcnt vmcnt(8)" ::: "memory");
      if (s == 2) asm volatile("s_waitcnt vmcnt(4)" ::: "memory");
      if (s == 3) { asm volatile("s_waitcnt vmcnt(0)" ::: "memory"); STAMP2(2); }
      v4u bf[4];
#pragma unroll
      for (int t = 0; t < 4; ++t) bf[t] = tr_frag(trs[4 * s + t], trs[4 * s + t] + 512);
      __builtin_amdgcn_sched_barrier(0);
#pragma unroll
      for (int t = 0; t < 4; ++t) acc[t] = mfma(af[s], bf[t], acc[t]);
      __builtin_amdgcn_sched_barrier(0);
    }
    {
      const float s0 = (g & 1) ? acc[1][0] : acc[0][0], s1 = (g & 1) ? acc[1][1] : acc[0][1];
      const float s2 = (g & 1) ? acc[3][0] : acc[2][0], s3 = (g & 1) ? acc[3][1] : acc[2][1];
      const v2u q0 = {U_((g & 2) ? s2 : s0), tag | np0}, q1 = {U_((g & 2) ? s3 : s1), tag | np0};
      __builtin_amdgcn_raw_buffer_store_b64(q0, rs, qoff, 0, SC1);
      __builtin_amdgcn_raw_buffer_store_b64(q1, rs, qoff, 4096, SC1);
    }
    STAMP(3);
    RBAR();
    while ((unsigned)__builtin_amdgcn_s_memtime() - t_start < TLOOK) __builtin_amdgcn_s_sleep(1);
    BAR();
    RBAR();
    const u16* wsrc; unsigned wv0, wstep;
    if (j < 16) {
      wsrc = p.wo + (size_t)((kvh * 4 + (j >> 2)) * 128) * 512 + (j & 3) * 128;
      wv0 = g * 1024 + (((c >> 1) ^ g) * 32) + (c & 1) * 16; wstep = 4096;
    } else {
      wsrc = p.w1 + (kvh * 7 + (j - 16)) * 32;
      wv0 = (lane >> 2) * (FF * 2) + (((pp >> 1) ^ (lane >> 5)) * 32) + (pp & 1) * 16; wstep = 16 * FF * 2;
    }
    const unsigned wx = j < 16 ? 128u : 0u;
    const unsigned wv1 = wv0 + wstep, wv2 = (wv0 ^ wx) + 2 * wstep, wv3 = (wv0 ^ wx) + 3 * wstep;
#define W4(i_) do { const int i4 = (i_) ^ ((dph & 7) * 4); const unsigned char* s_ = (const unsigned char*)wsrc + (size_t)i4 * wstep; const unsigned l_ = LB + L_W + i4 * 1024; \
    dma16(s_, wv0, l_); dma16(s_, wv1, l_ + 1024); dma16(s_, wv2, l_ + 2048); dma16(s_, wv3, l_ + 3072); } while (0)
    const int wn1 = j < 16 ? 32 : WN1F;
    RBAR();
    npo = __builtin_amdgcn_readfirstlane(LD(unsigned, LB + L_TAG + 44));
    __builtin_amdgcn_s_setprio(0);
    STAMP(4);
    if (j < 23) {
#pragma unroll 1
      for (int i = 0; i < wn1; i += 4) W4(i);
    }
    {
      const unsigned o_ml = c ? 0x7ffffff0u : pa_ml(kvh, g, b) + sp * 128;
      float mw, lw;
#pragma unroll 1
      for (;;) {
        const unsigned cnt = *(volatile LAS unsigned*)(LB + L_TAG + 4);
        mw = *(volatile LAS float*)(LB + L_MX + g * 16 + pp * 4);
        if (__builtin_amdgcn_readfirstlane(cnt) >= 4u) break;
        __builtin_amdgcn_s_sleep(1);
      }
      float M = vmax(mw, dpp<XOR1>(mw)); M = vmax(M, dpp<XOR2>(M));
      __builtin_amdgcn_raw_buffer_store_b64(v2u{U_(M), tag}, rs, o_ml, npo, SC1);
      const float fw = __builtin_amdgcn_exp2f(mw - M);
#pragma unroll 1
      for (;;) {
        const unsigned cnt = *(volatile LAS unsigned*)(LB + L_TAG + 16);
        lw = *(volatile LAS float*)(LB + L_LS + g * 16 + pp * 4);
        if (__builtin_amdgcn_readfirstlane(cnt) >= 4u) break;
        __builtin_amdgcn_s_sleep(1);
      }
      float S = lw * fw;
      S += dpp<XOR1>(S); S += dpp<XOR2>(S);
      __builtin_amdgcn_raw_buffer_store_b64(v2u{U_(S), tag}, rs, o_ml, npo + 2048, SC1);
    }
    if (j >= 16 && j < 23) {
      while (__builtin_amdgcn_readfirstlane(*(volatile LAS unsigned*)(LB + L_TAG + 12)) < 4u) __builtin_amdgcn_s_sleep(1);
#pragma unroll 1
      for (int i = WN1F; i < 32; i += 4) W4(i);
      {
        const u16* src2 = p.w2 + (size_t)((kvh * 7 + (j - 16)) * 32) * 512;
        const unsigned u0 = lane * 16, u1 = (u0 ^ 32u) + 1024u, u2 = (u0 ^ 64u) + 2048u, u3 = (u0 ^ 96u) + 3072u;
#pragma unroll 1
        for (int i = 0; i < 8; ++i) {
          const int i4 = i ^ (dph & 7);
          const unsigned char* s_ = (const unsigned char*)src2 + i4 * 4096; const unsigned l_ = LB + L_W + 32768 + i4 * 4096, x_ = (i4 & 2) * 64;
          dma16(s_, u0 ^ x_, l_); dma16(s_, u1 ^ x_, l_ + 1024); dma16(s_, u2 ^ x_, l_ + 2048); dma16(s_, u3 ^ x_, l_ + 3072);
        }
      }
    }
  } else {
    const int w = wave - 1;
    v4u kf[8], vf[8];
#define LDV(i) LDNT(vp + (pk0 ^ (i)) * 1024 + vo)
    unsigned vo = lane * 16;
    unsigned mk0 = (c & 2) ? 0u : ((c & 1) ? 0xffff0000u : 0x0000ffffu), mk1 = (c & 2) ? ((c & 1) ? 0xffff0000u : 0x0000ffffu) : 0u;
    unsigned a_q = LB + L_QS + (c >> 2) * QS_PITCH + g * 16;
    unsigned a_mw = LB + L_MX + (c >> 2) * 16 + w * 4, a_mr = LB + L_MX + g * 16, a_mo = LB + L_MX + g * 16 + w * 4;
    unsigned a_ow = LB + L_RED + w * RED_PITCH + g * 512 + c * 16;
    unsigned a_or = LB + L_RED + w * 512 + ((lane >> 1) & 1) * 256 + (lane >> 2) * 16 + (lane & 1) * 8;
    unsigned o_pa = pa_data(kvh, w, b) + sp * 512 + lane * 8;
    unsigned a_cnt = LB + L_TAG + 4, one = 1u;
    PIN(vo); PIN(one); PIN(mk0); PIN(mk1); PIN(a_q); PIN(a_mw); PIN(a_mr); PIN(a_mo); PIN(a_ow); PIN(a_or); PIN(o_pa); PIN(a_cnt);
    unsigned a_k = LB + L_K + w * 8192 + (c & 3) * 1024 + (c >> 2) * 256 + ((g ^ c) * 16);
    PIN(a_k);
#pragma unroll
    for (int i = 0; i < NV0; ++i) vf[i] = LDV(i);
    RBAR();
#pragma unroll
    for (int i = NV0; i < NV1; ++i) vf[i] = LDV(i);
    unsigned goff = WS_QG + kvh * 4 * 8192 + b * 4096 + w * 1024 + lane * 16;
    unsigned a_qw = LB + L_QS + w * QS_PITCH + lane * 4;
    PIN(goff); PIN(a_qw);
    asm volatile("s_waitcnt vmcnt(%0)" ::"i"(NV1) : "memory");
#pragma unroll
    for (int i = 0; i < 8; ++i) kf[i] = LD(v4u, (a_k ^ ((i & 3) * 64)) + (i >> 2) * 4096);
#pragma unroll
    for (int i = 0; i < 8; ++i) PIN(kf[i]);
    RBAR();
    v4u d[4];
#define QLOAD() _Pragma("unroll") for (int i = 0; i < 4; ++i) d[i] = __builtin_amdgcn_raw_buffer_load_b128(rs, goff, i * 8192, SC1)
    QLOAD();
    RBAR();
#pragma unroll
    for (int i = NV1; i < 8; ++i) vf[i] = LDV(i);
    tag = LD(unsigned, a_cnt - 4);
    PIN(tag);
    STAMP2(1);
    __builtin_amdgcn_s_setprio(2);
#define QBAD() ({ unsigned bad = 0; _Pragma("unroll") for (int i = 0; i < 4; ++i) bad = tagbad(bad, d[i], tag); ANY(bad >> 1); })
#define QPUT() do { LD(unsigned, a_qw) = pk_bf16((F_(d[0].x) + F_(d[1].x)) + (F_(d[2].x) + F_(d[3].x)), (F_(d[0].z) + F_(d[1].z)) + (F_(d[2].z) + F_(d[3].z))); \
    if (w == 0) asm volatile("s_mov_b64 exec, 1\n\tds_write_b32 %0, %1 offset:40\n\ts_mov_b64 exec, -1" ::"v"(a_cnt), "v"((d[0].y & d[1].y & d[2].y & d[3].y & 1u) << 13) : "memory"); } while (0)
    QPUT();
    if (__builtin_expect(QBAD(), 0)) {
#pragma unroll 1
      for (unsigned spin = 0; spin < SPIN_CAP && QBAD(); ++spin) {
        CBAR();
        QLOAD();
      }
      __builtin_amdgcn_s_waitcnt(0x0F70);
      QPUT();
    }
    BAR();
    STAMP(2);
    v4u qf[4];
#pragma unroll
    for (int s = 0; s < 4; ++s) qf[s] = LD(v4u, a_q + s * 64);
    npo = LD(unsigned, a_cnt + 40);
    __builtin_amdgcn_sched_barrier(0);
    f32x4 sc[2] = {};
#pragma unroll
    for (int s = 0; s < 4; ++s) { sc[0] = mfma(kf[s], qf[s], sc[0]); sc[1] = mfma(kf[4 + s], qf[s], sc[1]); __builtin_amdgcn_sched_barrier(0); }
    o_pa += npo;
    float sv[8];
#pragma unroll
    for (int i = 0; i < 4; ++i) {
      const unsigned r = pk_bf16(sc[i >> 1][(i & 1) * 2], sc[i >> 1][(i & 1) * 2 + 1]);
      const f32x2 t2 = f32x2{bf_lo(r), bf_hi(r)} * 0.08838834764831845f;
      const unsigned t = pk_bf16(t2[0], t2[1]);
      sv[2 * i] = bf_lo(t); sv[2 * i + 1] = bf_hi(t);
    }
    const float mw = xrows_max(vmax3(vmax3(sv[0], sv[1], sv[2]), vmax3(sv[3], sv[4], sv[5]), vmax(sv[6], sv[7]))) * LOG2E;
    asm volatile("s_mov_b64 exec, 0x1111\n\tds_write_b32 %0, %1\n\ts_mov_b64 exec, 1\n\tds_add_u32 %2, %3\n\ts_mov_b64 exec, -1"
                 ::"v"(a_mw), "v"(mw), "v"(a_cnt), "v"(one) : "memory");
    __builtin_amdgcn_sched_barrier(0);
    float e[8];
    f32x4 olo = {}, ohi = {};
    unsigned cnt2; f32x4 m0; float mo;
#pragma unroll
    for (int t = 0; t < 8; ++t) {
      e[t] = __builtin_amdgcn_exp2f(fmaf(sv[t], LOG2E, -mw));
      const unsigned pk = pk_bf16(e[t], e[t]);
      const v2u pa = v2u{pk & mk0, pk & mk1};
      olo = mfma16(pa, v2u{vf[t].x, vf[t].y}, olo); ohi = mfma16(pa, v2u{vf[t].z, vf[t].w}, ohi);
      if (t == 3) {
        __builtin_amdgcn_sched_barrier(0);
        cnt2 = *(volatile LAS unsigned*)(a_cnt); m0 = *(volatile LAS f32x4*)(a_mr); mo = *(volatile LAS float*)(a_mo);
        __builtin_amdgcn_sched_barrier(0);
      }
    }
    float fac = __builtin_amdgcn_exp2f(mo - vmax3(vmax(m0[0], m0[1]), m0[2], m0[3]));
    PIN(fac); PIN(olo); PIN(ohi);
    if (__builtin_expect(__builtin_amdgcn_readfirstlane(cnt2) < 4u, 0)) {
      do {
        cnt2 = *(volatile LAS unsigned*)(a_cnt);
        m0 = *(volatile LAS f32x4*)(a_mr);
      } while (__builtin_amdgcn_readfirstlane(cnt2) < 4u);
      fac = __builtin_amdgcn_exp2f(mo - vmax3(vmax(m0[0], m0[1]), m0[2], m0[3]));
    }
    STAMP2(10);
    LD(f32x4, a_ow) = olo * fac;
    LD(f32x4, a_ow + 256) = ohi * fac;
    asm volatile("s_mov_b64 exec, 1\n\tds_add_u32 %0, %1 offset:4\n\ts_mov_b64 exec, -1" ::"v"(a_cnt), "v"(one) : "memory");
    {
      const float lw = x32_sum(x16_sum(((e[0] + e[1]) + (e[2] + e[3])) + ((e[4] + e[5]) + (e[6] + e[7]))));
      asm volatile("s_mov_b64 exec, 0x1111\n\tds_write_b32 %0, %1 offset:128\n\ts_mov_b64 exec, 1\n\tds_add_u32 %2, %3 offset:12\n\ts_mov_b64 exec, -1"
                   ::"v"(a_mw), "v"(lw), "v"(a_cnt), "v"(one) : "memory");
    }
    STAMP2(4);
    {
      f32x2 r0, r1, r2, r3;
#pragma unroll 1
      for (;;) {
        const unsigned cnt = *(volatile LAS unsigned*)(a_cnt + 4);
        r0 = *(volatile LAS f32x2*)(a_or); r1 = *(volatile LAS f32x2*)(a_or + RED_PITCH);
        r2 = *(volatile LAS f32x2*)(a_or + 2 * RED_PITCH); r3 = *(volatile LAS f32x2*)(a_or + 3 * RED_PITCH);
        if (__builtin_amdgcn_readfirstlane(cnt) >= 4u) break;
      }
      const f32x2 a = (r0 + r1) + (r2 + r3);
      __builtin_amdgcn_raw_buffer_store_b64(v2u{pk_bf16(a[0], a[1]), tag}, rs, o_pa, 0, SC1);
    }
    asm volatile("s_mov_b64 exec, 1\n\tds_add_u32 %0, %1 offset:8\n\ts_mov_b64 exec, -1" ::"v"(a_cnt), "v"(one) : "memory");
    STAMP(5);
  }

  if (j < 16) {
    const int h = kvh * 4 + (j >> 2), cq = j & 3;
    if (wave == 0) {
      const unsigned mlo = pa_ml(kvh, j >> 2, g & 1) + c * 128 + npo;
      v2u dm, dl;
      asm volatile("buffer_load_dwordx2 %0, %2, %3, 0 offen sc1\n\tbuffer_load_dwordx2 %1, %2, %3, 0 offen offset:2048 sc1" : "=&v"(dm), "=&v"(dl) : "v"(mlo), "s"(rs) : "memory");
      asm volatile("s_waitcnt vmcnt(2)" ::: "memory");
      if (lane == 0) *(volatile LAS unsigned*)(LB + L_TAG + 20) = 1u;
      STAMP2(9);
      asm volatile("s_waitcnt vmcnt(0)" : "+v"(dm), "+v"(dl)::"memory");
#pragma unroll 1
      for (unsigned spin = 0; spin < SPIN_CAP && ANY(dm.y ^ tag); ++spin) {
        CBAR();
        dm = __builtin_amdgcn_raw_buffer_load_b64(rs, mlo, 0, SC1); dl = __builtin_amdgcn_raw_buffer_load_b64(rs, mlo + 2048, 0, SC1);
      }
      v2u dl2 = __builtin_amdgcn_raw_buffer_load_b64(rs, mlo + 2048, 0, SC1);
      const float m = F_(dm.x);
      float M = vmax(m, dpp<XOR1>(m)); M = vmax(M, dpp<XOR2>(M)); M = vmax(M, dpp<HMIR>(M)); M = vmax(M, dpp<ROR8>(M));
      const float ex = __builtin_amdgcn_exp2f(m - M);
      const unsigned wb = pk_bf16(ex, ex);
      if (g < 2) *(volatile LAS unsigned*)(LB + L_WN + g * 64 + (c & 3) * 16 + (c >> 2) * 4) = wb;
      if (lane == 0) *(volatile LAS unsigned*)(LB + L_TAG + 24) = 1u;
      STAMP2(8);
      if (ANY(dl.y ^ tag)) {
        dl = dl2;
#pragma unroll 1
        for (unsigned spin = 0; spin < SPIN_CAP && ANY(dl.y ^ tag); ++spin) { CBAR(); dl = __builtin_amdgcn_raw_buffer_load_b64(rs, mlo + 2048, 0, SC1); }
      }
      float S = bf_lo(wb) * F_(dl.x);
      S += dpp<XOR1>(S); S += dpp<XOR2>(S); S += dpp<HMIR>(S); S += dpp<ROR8>(S);
      if (g < 2 && c == 0) *(volatile LAS float*)(LB + L_TAG + 32 + g * 4) = __builtin_amdgcn_rcpf(S);
      if (lane == 0) *(volatile LAS unsigned*)(LB + L_TAG + 28) = 1u;
      STAMP(6);
      STAMP_FLUSH();
      return;
    }
    const int w = wave - 1;
    const int ob = w >> 1, dh = w & 1;
    unsigned off = pa_data(kvh, j >> 2, ob) + g * 512 + dh * 256 + c * 16 + npo;
    v4u d[4];
    unsigned a_ow = LB + L_ACT + ob * ACT_PITCH + dh * 128 + c * 8;
    unsigned a_or = LB + L_ACT + (c & 1) * ACT_PITCH + g * 16;
    unsigned o_pb = g > 1 ? 0x7ffffff0u : pb_inbox(cq * 8 + 2 * w + g) + h * 128 + c * 8;
    unsigned a_wn = LB + L_WN + ob * 64 + g * 16, a_fl = LB + L_TAG + 20, a_inv = LB + L_TAG + 32;
    const int q4 = (lane >> 2) & 3, pp = lane & 3;
    unsigned a_wf = LB + L_W + (8 * g + q4) * 256 + (((2 * w) ^ (q4 | ((g & 1) << 2))) * 32) + pp * 8;
    unsigned mk0 = (c & 2) ? 0u : ((c & 1) ? 0xffff0000u : 0x0000ffffu), mk1 = (c & 2) ? ((c & 1) ? 0xffff0000u : 0x0000ffffu) : 0u;
    unsigned one = 1u;
    PIN(off); PIN(a_ow); PIN(a_or); PIN(o_pb); PIN(a_wn); PIN(a_fl); PIN(a_wf); PIN(a_inv); PIN(mk0); PIN(mk1); PIN(one);
#define OLOAD() _Pragma("unroll") for (int i = 0; i < 4; ++i) d[i] = __builtin_amdgcn_raw_buffer_load_b128(rs, off + (i & 1) * 2048, (i >> 1) * 4096, SC1)
#define OOK() ({ unsigned bad = 0; _Pragma("unroll") for (int i = 0; i < 4; ++i) bad = tagbad(bad, d[i], tag); !ANY(bad); })
    OLOAD();
    while (__builtin_amdgcn_readfirstlane(*(volatile LAS unsigned*)(a_fl)) == 0u) {}
    CBAR();
    v4u wf[8];
#pragma unroll
    for (int s = 0; s < 8; ++s) wf[s] = tr_frag((a_wf ^ ((s & 1) * 32)) + (s >> 1) * 8192, (a_wf ^ ((s & 1) * 32)) + (s >> 1) * 8192 + 1024);
    v4u wn; unsigned fl;
#pragma unroll 1
    for (unsigned spin = 0; spin < SPIN_CAP; ++spin) {
      fl = *(volatile LAS unsigned*)(a_fl + 4);
      wn = *(volatile LAS v4u*)(a_wn);
      if (OOK()) break;
      CBAR(); OLOAD();
    }
    while (__builtin_amdgcn_readfirstlane(fl) == 0u) {
      fl = *(volatile LAS unsigned*)(a_fl + 4);
      wn = *(volatile LAS v4u*)(a_wn);
    }
    v2u wa[4];
#pragma unroll
    for (int i = 0; i < 4; ++i) wa[i] = v2u{wn[i] & mk0, wn[i] & mk1};
    STAMP(6);
    {
      f32x4 o = {};
#pragma unroll
      for (int i = 0; i < 4; ++i) o = mfma16(wa[i], v2u{d[i].x, d[i].z}, o);
      if (g == 0) LD(v2u, a_ow) = v2u{pk_bf16(o[0], o[1]), pk_bf16(o[2], o[3])};
    }
    asm volatile("s_mov_b64 exec, 1\n\tds_add_u32 %0, %1 offset:28\n\ts_mov_b64 exec, -1" ::"v"(a_fl), "v"(one) : "memory");
    f32x4 acc0 = {}, acc1 = {};
    v4u of[4];
#pragma unroll 1
    for (;;) {
      const unsigned cnt = *(volatile LAS unsigned*)(a_fl + 28);
#pragma unroll
      for (int s = 0; s < 4; ++s) of[s] = *(volatile LAS v4u*)(a_or + s * 64);
      if (__builtin_amdgcn_readfirstlane(cnt) >= 4u) break;
    }
    unsigned fi = *(volatile LAS unsigned*)(a_fl + 8);
    f32x2 inv = *(volatile LAS f32x2*)(a_inv);
    __builtin_amdgcn_sched_barrier(0);
#pragma unroll
    for (int s = 0; s < 4; ++s) { acc0 = mfma(of[s], wf[2 * s], acc0); acc1 = mfma(of[s], wf[2 * s + 1], acc1); }
    float y0 = g ? acc1[0] : acc0[0], y1 = g ? acc1[1] : acc0[1];
    PIN(y0); PIN(y1);
    while (__builtin_expect(__builtin_amdgcn_readfirstlane(fi) == 0u, 0)) { fi = *(volatile LAS unsigned*)(a_fl + 8); inv = *(volatile LAS f32x2*)(a_inv); }
    __builtin_amdgcn_raw_buffer_store_b64(v2u{pk_bf16(y0 * inv[0], y1 * inv[1]), tag}, rs, o_pb, 0, SC1);
    STAMP(7);
  } else if (j < 23) {
    const int f = kvh * 7 + (j - 16);
    if (wave == 0) {
      asm volatile("s_waitcnt vmcnt(0)" ::: "memory");
      STAMP(6);
      RBAR();
      RBAR();
      unsigned a_h = LB + L_H + ((lane >> 2) & 1) * H_PITCH + (lane >> 3) * 32 + (lane & 3) * 8;
      unsigned a_st = LB + L_WN + ((lane >> 2) & 1) * 8;
      PIN(a_h); PIN(a_st);
      v4u d[4];
      poll<4, 1024>(rs, WS_HG + (kvh & 1) * 16384 + npo + ((kvh >> 1) & 1) * 4096 + lane * 16, tag, d);
      STAMP(7);
#pragma unroll
      for (int i = 0; i < 4; ++i) LD(v2u, a_h + i * 256) = v2u{d[i].x, d[i].z};
      BAR();
      PIN(d[0]); PIN(d[1]); PIN(d[2]); PIN(d[3]);
      float s0 = 0.f, s1 = 0.f, q0 = 0.f, q1 = 0.f;
#pragma unroll
      for (int i = 0; i < 4; ++i) {
        s0 = dot2(d[i].x, 0x3f803f80u, s0); s1 = dot2(d[i].z, 0x3f803f80u, s1);
        q0 = dot2(d[i].x, d[i].x, q0); q1 = dot2(d[i].z, d[i].z, q1);
      }
      float t = x16_sum2(s0 + s1, q0 + q1);
      t += dpp<XOR1>(t); t += dpp<XOR2>(t); t += dpp<ROR8>(t);
      t = x32_sum(t) * (1.0f / 512.0f);
      auto u = __builtin_amdgcn_permlane16_swap(U_(t), U_(t), false, false);
      const float mean = F_(u[0]);
      const float var = fmaxf(fmaf(-mean, mean, F_(u[1])), 0.f);
      const float inv = __builtin_amdgcn_rcpf(rbf(__builtin_amdgcn_sqrtf(rbf(rbf(var) + 1e-5f))));
      LD(f32x2, a_st) = f32x2{rbf(mean), inv};
      BAR();
      STAMP(8);
      STAMP_FLUSH();
      return;
    }
    const int w = wave - 1;
    float bias1 = bf_lo(p.b1[f * 32 + (lane & 31)]);
    RBAR();
    v4u w1f[8], w2f[8];
    {
      const int q4 = (lane >> 2) & 3, pp = lane & 3;
      const unsigned b1a = LB + L_W + (128 * w + 8 * g + q4) * 64 + ((g & 1) * 32) + pp * 8;
#pragma unroll
      for (int s = 0; s < 8; ++s) w1f[s] = tr_frag((b1a ^ ((s & 1) * 32)) + (s >> 1) * 2048, (b1a ^ ((s & 1) * 32)) + (s >> 1) * 2048 + 256);
      const unsigned b2a = LB + L_W + 32768 + (8 * g + q4) * 1024 + w * 256 + ((q4 | ((g & 1) << 2)) * 32) + pp * 8;
#pragma unroll
      for (int t = 0; t < 8; ++t) w2f[t] = tr_frag(b2a ^ (t * 32), (b2a ^ (t * 32)) + 4096);
    }
    const f32x4 z4 = {};
    unsigned a_zw = LB + L_ZP + ((g & 1) * 32 + (g >> 1) * 16 + c) * 16 + w * 4;
    unsigned a_zr = LB + L_ZP + lane * 16;
    unsigned a_ha = LB + L_H + (c & 1) * H_PITCH + w * 256 + g * 16;
    unsigned a_st = LB + L_WN + (lane >> 5) * 8;
    unsigned a_gw = LB + L_GA + w * 160 + (lane >> 5) * 80 + (lane & 31) * 2, a_gr = LB + L_GA + w * 160 + (c & 1) * 80 + g * 16;
    unsigned o_pd = WS_PD + (8 * w + g) * 8192 + f * 128 + c * 8;
    float csum;
    {
      const v4u ones = {0x3f803f80u, 0x3f803f80u, 0x3f803f80u, 0x3f803f80u};
      f32x4 c0 = z4, c1 = z4;
#pragma unroll
      for (int s = 0; s < 4; ++s) { c0 = mfma(ones, w1f[2 * s], c0); c1 = mfma(ones, w1f[2 * s + 1], c1); }
      LD(float, a_zw) = (g & 2) ? c1[0] : c0[0];
      BAR();
      const f32x4 a = LD(f32x4, a_zr);
      csum = (a[0] + a[1]) + (a[2] + a[3]);
      asm volatile("" : "+v"(csum), "+v"(w2f[0]), "+v"(w2f[1]), "+v"(w2f[2]), "+v"(w2f[3]), "+v"(w2f[4]), "+v"(w2f[5]), "+v"(w2f[6]), "+v"(w2f[7]), "+v"(bias1));
      PIN(a_zw); PIN(a_zr); PIN(a_ha); PIN(a_st); PIN(a_gw); PIN(a_gr); PIN(o_pd);
    }
    BAR();
    STAMP2(7);
    {
      v4u ha[4];
#pragma unroll
      for (int s = 0; s < 4; ++s) ha[s] = LD(v4u, a_ha + s * 64);
      __builtin_amdgcn_sched_barrier(0);
      f32x4 z0 = z4, z1 = z4;
#pragma unroll
      for (int s = 0; s < 4; ++s) { z0 = mfma(ha[s], w1f[2 * s], z0); z1 = mfma(ha[s], w1f[2 * s + 1], z1); }
      const float t0 = (g & 1) ? z0[1] : z0[0], t1 = (g & 1) ? z1[1] : z1[0];
      LD(float, a_zw) = (g & 2) ? t1 : t0;
    }
    BAR();
    {
      const f32x4 a = LD(f32x4, a_zr);
      const f32x2 st = LD(f32x2, a_st);
      const float zr = (a[0] + a[1]) + (a[2] + a[3]);
      const float z = rbf(rbf(fmaf(-st[0], csum, zr) * st[1]) + bias1);
      const float t = fmaf(z * z, 0.044715f * 0.7978845608028654f * 2.f * LOG2E, 0.7978845608028654f * 2.f * LOG2E);
      const float e = __builtin_amdgcn_exp2f(z * t);
      const float gl = fmaf(-z, __builtin_amdgcn_rcpf(e + 1.0f), z);
      LD(u16, a_gw) = (u16)pk_bf16(gl, gl);
    }
    CBAR();
    const v4u ga = LD(v4u, a_gr);
#pragma unroll
    for (int u = 0; u < 2; ++u) {
      const f32x4 y0 = mfma(ga, w2f[4 * u], z4), y1 = mfma(ga, w2f[4 * u + 1], z4), y2 = mfma(ga, w2f[4 * u + 2], z4), y3 = mfma(ga, w2f[4 * u + 3], z4);
      const unsigned p0 = pk_bf16(y0[0], y0[1]), p1 = pk_bf16(y1[0], y1[1]), p2 = pk_bf16(y2[0], y2[1]), p3 = pk_bf16(y3[0], y3[1]);
      __builtin_amdgcn_raw_buffer_store_b64(v2u{(g & 2) ? ((g & 1) ? p3 : p2) : ((g & 1) ? p1 : p0), tag}, rs, o_pd, u * 4 * 8192, SC1);
    }
    STAMP(8);
  } else if (j < 31 && wave == 0) {
    const unsigned np = npo >> 13;
    const unsigned role = claim_role(p.ws, (((j - 23) * 4 + (kvh & 3)) * 2) | np, tag);
    const int r = role & 31, n = lane & 7, pg = lane >> 3;
    if (j < 27) {
      unsigned xr = *(const unsigned*)(p.x + (g & 1) * 512 + r * 16 + 2 * n);
      unsigned o_hg = WS_HG + ((lane >> 3) & 1) * 8192 + (g >> 1) * 4096 + r * 128 + ((g & 1) * 8 + n) * 8;
      PIN(xr); PIN(o_hg);
      STAMP2(9);
      const f32x4 a = poll_sum<4>(rs, pb_inbox(r) + lane * 16, tag);
      STAMP(6);
      float t = x32_sum2(x16_sum2(a[0], a[1]), x16_sum2(a[2], a[3]));
      t += dpp<ROR8>(t);
      auto sw = __builtin_amdgcn_permlane32_swap(U_(t), U_(t), false, false);
      const unsigned ar = pk_bf16(F_(sw[0]), F_(sw[1]));
      const unsigned pk = pk_bf16(bf_lo(xr) + bf_lo(ar), bf_hi(xr) + bf_hi(ar));
      __builtin_amdgcn_raw_buffer_store_b64(v2u{pk, tag}, rs, o_hg, 0, SC1);
      __builtin_amdgcn_raw_buffer_store_b64(v2u{pk, tag}, rs, o_hg, 16384, SC1);
      __builtin_amdgcn_raw_buffer_store_b64(v2u{pk, tag}, rs, o_hg, 32768, SC1);
      if (r == 0 && lane == 0) __builtin_amdgcn_raw_buffer_store_b32(tag, rs, WS_EPOCH, 0, SC1);
      STAMP(7);
    } else {
      unsigned b2r = *(const unsigned*)(p.b2 + r * 16 + 2 * n);
      v2u hg;
      const unsigned hoff = WS_HG + 32768 + np * 8192 + r * 128 + ((g & 1) * 8 + n) * 8;
#pragma unroll 1
      for (unsigned spin = 0; spin < SPIN_CAP; ++spin) {
        CBAR();
        hg = __builtin_amdgcn_raw_buffer_load_b64(rs, hoff, 0, SC1);
        if (__all(hg.y == tag)) break;
        __builtin_amdgcn_s_sleep(2);
      }
      unsigned o_out = ((g & 1) * 512 + r * 16 + 2 * n) * 2;
      PIN(b2r); PIN(o_out);
      const f32x4 a = poll_sum<7>(rs, WS_PD + r * 8192 + lane * 16, tag);
      STAMP(8);
      float t = x32_sum2(x16_sum2(a[0], a[1]), x16_sum2(a[2], a[3]));
      t += dpp<ROR8>(t);
      auto sw = __builtin_amdgcn_permlane32_swap(U_(t), U_(t), false, false);
      const unsigned ar = pk_bf16(F_(sw[0]), F_(sw[1]));
      const unsigned yr = pk_bf16(bf_lo(ar) + bf_lo(b2r), bf_hi(ar) + bf_hi(b2r));
      const unsigned pk = pk_bf16(bf_lo(hg.x) + bf_lo(yr), bf_hi(hg.x) + bf_hi(yr));
      if ((lane & 40) == 0) *(unsigned*)((unsigned char*)p.out + o_out) = pk;
      STAMP(9);
    }
  }
  STAMP_FLUSH();
}

}

extern "C" void kernel_launch(void* const* d_in, const int* in_sizes, int n_in,
                              void* d_out, int out_size, void* d_ws, size_t ws_size,
                              hipStream_t stream) {
  Params p;
  p.x = (const u16*)d_in[0]; p.kc = (const u16*)d_in[1]; p.vc = (const u16*)d_in[2];
  p.wq = (const u16*)d_in[3]; p.wo = (const u16*)d_in[4];
  p.w1 = (const u16*)d_in[6]; p.b1 = (const u16*)d_in[7]; p.w2 = (const u16*)d_in[8]; p.b2 = (const u16*)d_in[9];
  p.out = (u16*)d_out; p.ws = (unsigned char*)(((uintptr_t)d_ws + 16383) & ~(uintptr_t)16383);
  (void)hipFuncSetAttribute((const void*)fused_decode, hipFuncAttributeMaxDynamicSharedMemorySize, LDS_BYTES);
  hipLaunchKernelGGL(fused_decode, dim3(256), dim3(NTHR), LDS_BYTES, stream, p);
}
```

```cpp
#include <hip/hip_runtime.h>
#include <hip/hip_bf16.h>
#include <stdint.h>

typedef unsigned short u16;
typedef unsigned long long u64;
typedef unsigned v4u __attribute__((ext_vector_type(4)));
typedef unsigned v2u __attribute__((ext_vector_type(2)));
typedef float f32x4 __attribute__((ext_vector_type(4)));
typedef float f32x2 __attribute__((ext_vector_type(2)));
typedef short bf16x8 __attribute__((ext_vector_type(8)));
typedef short bf16x4 __attribute__((ext_vector_type(4)));
typedef __bf16 bf16x2 __attribute__((ext_vector_type(2)));
typedef short v4i16 __attribute__((ext_vector_type(4)));
#define LAS __attribute__((address_space(3)))
#define GAS __attribute__((address_space(1)))

namespace {

constexpr int SK = 8192, WIN = 2048, FF = 1792;
constexpr int NTHR = 320;
#ifndef NV0
#define NV0 1
#endif
#ifndef NV1
#define NV1 5
#endif
#define WN1F 16
#define TLOOK 5400
#define CODE_BYTES 12480
constexpr unsigned SPIN_CAP = 1u << 18;
constexpr float LOG2E = 1.4426950408889634f;

constexpr unsigned WS_EPOCH = 0;
constexpr unsigned WS_CLAIM = 1024;
constexpr unsigned WS_CAL = 16384;
constexpr unsigned WS_HG = WS_CAL + 65536;
constexpr unsigned WS_PB = WS_HG + 3 * 16384;
constexpr unsigned WS_PD = WS_PB + 8 * 16384;
constexpr unsigned WS_PA = WS_PD + 32 * 8192;
constexpr unsigned WS_QG = WS_PA + 8 * 9 * 16384;
constexpr unsigned WS_END = WS_QG + 8 * 4 * 8192;
__host__ __device__ constexpr unsigned pa_data(unsigned kvh, unsigned hh, unsigned b) { return WS_PA + (kvh * 9 + hh * 2 + b) * 16384; }
__host__ __device__ constexpr unsigned pa_ml(unsigned kvh, unsigned hh, unsigned b) { return WS_PA + (kvh * 9 + 8) * 16384 + b * 4096 + hh * 8; }
__host__ __device__ constexpr unsigned pb_inbox(unsigned r) { return WS_PB + (r >> 2) * 16384 + (r & 1) * 8192 + ((r >> 1) & 1) * 4096; }

constexpr int L_WQ = 0;
constexpr int RED_PITCH = 2048;
constexpr int L_RED = 0;
constexpr int L_W = 17408;
constexpr int L_K = L_W + 32768;
constexpr int L_ACT = L_W + 65536;
constexpr int ACT_PITCH = 256 + 64;
constexpr int L_QS = L_ACT + 2 * ACT_PITCH;
constexpr int QS_PITCH = 256 + 64;
constexpr int L_MX = L_QS + 4 * QS_PITCH;
constexpr int L_LS = L_MX + 128;
constexpr int L_WN = L_LS + 128;
constexpr int L_ZP = L_WN + 128;
constexpr int L_GA = L_ZP + 64 * 48;
constexpr int L_H = L_GA + 8 * 160;
constexpr int H_PITCH = 1024 + 64;
constexpr int L_TAG = L_H + 2 * H_PITCH;
constexpr int LDS_BYTES = L_TAG + 64;
static_assert(8 * RED_PITCH <= L_W, "lds map");
static_assert(LDS_BYTES > 81920, "one workgroup per CU");

struct Params {
  const u16* x; const u16* kc; const u16* vc; const u16* wq; const u16* wo;
  const u16* w1; const u16* b1; const u16* w2; const u16* b2;
  u16* out; unsigned char* ws;
};

#define F_(v) __uint_as_float(v)
#define U_(v) __float_as_uint(v)
__device__ __forceinline__ float bf_lo(unsigned d) { return F_(d << 16); }
__device__ __forceinline__ float bf_hi(unsigned d) { return F_(d & 0xffff0000u); }
__device__ __forceinline__ unsigned pk_bf16(float lo, float hi) {
  return __builtin_bit_cast(unsigned, __builtin_convertvector(f32x2{lo, hi}, bf16x2));
}
__device__ __forceinline__ float rbf(float f) { return bf_lo(pk_bf16(f, f)); }
__device__ __forceinline__ f32x4 mfma(v4u a, v4u b, f32x4 c) {
  return __builtin_amdgcn_mfma_f32_16x16x32_bf16(__builtin_bit_cast(bf16x8, a), __builtin_bit_cast(bf16x8, b), c, 0, 0, 0);
}
__device__ __forceinline__ f32x4 mfma16(v2u a, v2u b, f32x4 c) {
  return __builtin_amdgcn_mfma_f32_16x16x16bf16_1k(__builtin_bit_cast(bf16x4, a), __builtin_bit_cast(bf16x4, b), c, 0, 0, 0);
}
__device__ __forceinline__ float dot2(unsigned a, unsigned b, float acc) {
  return __builtin_amdgcn_fdot2_f32_bf16(__builtin_bit_cast(bf16x2, a), __builtin_bit_cast(bf16x2, b), acc, false);
}
template <int CTRL> __device__ __forceinline__ float dpp(float x) {
  return __builtin_bit_cast(float, __builtin_amdgcn_mov_dpp(__builtin_bit_cast(int, x), CTRL, 0xf, 0xf, true));
}
constexpr int XOR1 = 0xB1, XOR2 = 0x4E, HMIR = 0x141, ROR8 = 0x128;
__device__ __forceinline__ float x16_sum(float x) {
  auto s = __builtin_amdgcn_permlane16_swap(U_(x), U_(x), false, false);
  return F_(s[0]) + F_(s[1]);
}
__device__ __forceinline__ float x32_sum(float x) {
  auto s = __builtin_amdgcn_permlane32_swap(U_(x), U_(x), false, false);
  return F_(s[0]) + F_(s[1]);
}
__device__ __forceinline__ float x16_sum2(float a, float b) {
  auto s = __builtin_amdgcn_permlane16_swap(U_(a), U_(b), false, false);
  return F_(s[0]) + F_(s[1]);
}
__device__ __forceinline__ float x32_sum2(float a, float b) {
  auto s = __builtin_amdgcn_permlane32_swap(U_(a), U_(b), false, false);
  return F_(s[0]) + F_(s[1]);
}
__device__ __forceinline__ float vmax(float a, float b) { float r; asm("v_max_f32 %0, %1, %2" : "=v"(r) : "v"(a), "v"(b)); return r; }
__device__ __forceinline__ float vmax3(float a, float b, float c) { float r; asm("v_max3_f32 %0, %1, %2, %3" : "=v"(r) : "v"(a), "v"(b), "v"(c)); return r; }
__device__ __forceinline__ float xrows_max(float x) {
  auto s = __builtin_amdgcn_permlane16_swap(U_(x), U_(x), false, false);
  x = vmax(F_(s[0]), F_(s[1]));
  auto t = __builtin_amdgcn_permlane32_swap(U_(x), U_(x), false, false);
  return vmax(F_(t[0]), F_(t[1]));
}
#define BAR() asm volatile("s_waitcnt lgkmcnt(0)\n\ts_barrier" ::: "memory")
#define RBAR() asm volatile("s_barrier" ::: "memory")
#define CBAR() asm volatile("" ::: "memory")
#define PIN(x) asm volatile("" : "+v"(x))
#define LDNT(p) __builtin_nontemporal_load((const v4u*)(p))
#define SC1 16
#define LD(T, a) (*(LAS T*)(a))

__device__ __forceinline__ void dma16(const void* sbase, unsigned voff, unsigned ldsaddr) {
  asm volatile("s_mov_b32 m0, %2\n\ts_nop 0\n\tglobal_load_lds_dwordx4 %0, %1 nt" ::"v"(voff), "s"(sbase), "s"(ldsaddr) : "memory");
}
__device__ __forceinline__ v4u tr_frag(unsigned a0, unsigned a1) {
  v2u a = __builtin_bit_cast(v2u, __builtin_amdgcn_ds_read_tr16_b64_v4i16((LAS v4i16*)a0));
  v2u b = __builtin_bit_cast(v2u, __builtin_amdgcn_ds_read_tr16_b64_v4i16((LAS v4i16*)a1));
  return v4u{a.x, a.y, b.x, b.y};
}
__device__ __forceinline__ v4u ldg(__amdgpu_buffer_rsrc_t rs, unsigned off) { return __builtin_amdgcn_raw_buffer_load_b128(rs, off, 0, SC1); }

__device__ __forceinline__ void cal_issue(const unsigned char* ws) {
  const unsigned o = WS_CAL + (blockIdx.x >> 6) * 16384 + (blockIdx.x & 63) * 128;
  asm volatile("s_mov_b64 exec, 1\n\tglobal_store_dword %0, %0, %2 sc1\n\tglobal_store_dword %1, %0, %2 sc1\n\ts_mov_b64 exec, -1"
               ::"v"(o), "v"(o + 8192), "s"(ws) : "memory");
}
__device__ __forceinline__ unsigned cal_result() {
  u64 t1, t2;
  asm volatile("s_waitcnt vmcnt(1)\n\ts_memtime %0\n\ts_waitcnt vmcnt(0)\n\ts_memtime %1\n\ts_waitcnt lgkmcnt(0)" : "=&s"(t1), "=&s"(t2)::"memory");
  return ((unsigned)t2 - (unsigned)t1) > 100u ? 0u : 1u;
}
__device__ __forceinline__ unsigned claim_role(unsigned char* ws, unsigned role, unsigned tag) {
  unsigned old = 0;
  if ((threadIdx.x & 63) == 0) old = __hip_atomic_exchange((GAS unsigned*)(ws + WS_CLAIM + role * 128), tag, __ATOMIC_RELAXED, __HIP_MEMORY_SCOPE_AGENT);
  return __builtin_amdgcn_readfirstlane(old) == tag ? role ^ 1u : role;
}

__device__ __forceinline__ unsigned tagbad(unsigned bad, v4u d, unsigned tag) {
  return __builtin_amdgcn_bitop3_b32(__builtin_amdgcn_bitop3_b32(bad, d.y, tag, 0xF6), d.w, tag, 0xF6);
}
#define ANY(bad) (__builtin_amdgcn_ballot_w64((bad) != 0u) != 0ull)
template <int N, int STRIDE>
__device__ __forceinline__ void poll(__amdgpu_buffer_rsrc_t rs, unsigned off, unsigned tag, v4u (&d)[N]) {
  unsigned n = SPIN_CAP;
#pragma unroll 1
  for (;;) {
    asm volatile("" : "+s"(n)::"memory");
#pragma unroll
    for (int i = 0; i < N; ++i) d[i] = __builtin_amdgcn_raw_buffer_load_b128(rs, off + (i & 3) * STRIDE, (i >> 2) * 4 * STRIDE, SC1);
    unsigned bad = 0;
#pragma unroll
    for (int i = 0; i < N; ++i) bad = tagbad(bad, d[i], tag);
    if (!ANY(bad)) break;
    if (--n == 0u) break;
  }
}

template <int N>
__device__ __forceinline__ f32x4 poll_sum(__amdgpu_buffer_rsrc_t rs, unsigned off, unsigned tag) {
  unsigned n = SPIN_CAP, klo = 0x00003f80u, khi = 0x3f800000u;
  PIN(klo); PIN(khi);
  f32x4 s;
#pragma unroll 1
  for (;;) {
    asm volatile("" : "+s"(n)::"memory");
    v4u d[N];
#pragma unroll
    for (int i = 0; i < N; ++i) d[i] = __builtin_amdgcn_raw_buffer_load_b128(rs, off + (i & 3) * 1024, (i >> 2) * 4096, SC1);
    unsigned bad = 0;
    s = f32x4{0.f, 0.f, 0.f, 0.f};
#pragma unroll
    for (int i = 0; i < N; ++i) {
      s[0] = dot2(d[i].x, klo, s[0]); s[1] = dot2(d[i].x, khi, s[1]);
      s[2] = dot2(d[i].z, klo, s[2]); s[3] = dot2(d[i].z, khi, s[3]);
      bad = tagbad(bad, d[i], tag);
    }
    PIN(s);
    if (!ANY(bad)) break;
    if (--n == 0u) break;
  }
  return s;
}
__device__ __forceinline__ float half_sum(float x) {
  x += dpp<XOR1>(x); x += dpp<XOR2>(x); x += dpp<HMIR>(x); x += dpp<ROR8>(x);
  return x16_sum(x);
}
__device__ __forceinline__ void ln_stats(v4u a, v4u b, float& mb, float& inv) {
  float s0 = 0.f, s1 = 0.f;
#pragma unroll
  for (int i = 0; i < 4; ++i) { s0 = dot2(a[i], 0x3f803f80u, s0); s1 = dot2(b[i], 0x3f803f80u, s1); }
  const float mean = half_sum(s0 + s1) * (1.0f / 512.0f);
  f32x2 q0 = {0.f, 0.f}, q1 = {0.f, 0.f};
#pragma unroll
  for (int i = 0; i < 4; ++i) {
    const f32x2 u = f32x2{bf_lo(a[i]), bf_hi(a[i])} - mean, v = f32x2{bf_lo(b[i]), bf_hi(b[i])} - mean;
    q0 = u * u + q0; q1 = v * v + q1;
  }
  q0 += q1;
  const float var = half_sum(q0[0] + q0[1]) * (1.0f / 512.0f);
  mb = rbf(mean);
  inv = __builtin_amdgcn_rcpf(rbf(__builtin_amdgcn_sqrtf(rbf(rbf(var) + 1e-5f))));
}

__global__ void __launch_bounds__(NTHR, 1) __attribute__((aligned(4096))) fused_decode(Params p) {
  extern __shared__ __attribute__((aligned(16))) unsigned char lds_raw[];
  u64 pc;
  asm volatile("s_getpc_b64 %0" : "=s"(pc));
  const unsigned LB = (unsigned)(size_t)(LAS unsigned char*)lds_raw;
  const int tid = threadIdx.x, lane = tid & 63, hwave = __builtin_amdgcn_readfirstlane(tid >> 6);
  const int wave = hwave == 4 ? 0 : hwave + 1;
  const int kvh = blockIdx.x & 7, j = blockIdx.x >> 3;
  const int g = lane >> 4, c = lane & 15;
  unsigned t_start = 0;
  const __amdgpu_buffer_rsrc_t rs = __builtin_amdgcn_make_buffer_rsrc((void*)p.ws, 0, WS_END, 0x00020000);
  const int b = j >> 4, sp = j & 15;
  unsigned tag, npo;

  const int rg = j >> 3, cg = j & 7;
  const int dph = j + kvh * 5;
  unsigned ep; v4u x0, x1; v2u xs;
  if (wave > 1) {
    unsigned o = (j * 3 + wave - 2) * 192, t0, t1, t2;
    o = o < CODE_BYTES - 192 ? o : CODE_BYTES - 192;
    asm volatile("s_load_dword %0, %3, 0x0\n\ts_load_dword %1, %3, 0x40\n\ts_load_dword %2, %3, 0x80\n\ts_waitcnt lgkmcnt(0)"
                 : "=&s"(t0), "=&s"(t1), "=&s"(t2) : "s"((pc & ~255ull) + o));
  }
  asm volatile("" ::"s"(p.ws), "s"(p.kc), "s"(p.out));
  if (wave == 1) { cal_issue(p.ws); const unsigned np0 = cal_result(); if (lane == 0) LD(unsigned, LB + L_TAG + 40) = np0; asm volatile("s_waitcnt lgkmcnt(0)" ::: "memory"); }
  if (wave == 0) {
    __builtin_amdgcn_s_setprio(3);
    asm volatile("global_load_dword %0, %1, %2 sc1" : "=v"(ep) : "v"(WS_EPOCH), "s"(p.ws) : "memory");
    asm volatile("global_load_dwordx4 %0, %1, %2" : "=v"(x0) : "v"(lane * 32), "s"(p.x) : "memory");
    asm volatile("global_load_dwordx4 %0, %1, %2 offset:16" : "=v"(x1) : "v"(lane * 32), "s"(p.x) : "memory");
    asm volatile("global_load_dwordx2 %0, %1, %2" : "=v"(xs) : "v"((lane >> 5) * 1024 + rg * 256 + (lane & 31) * 8), "s"(p.x) : "memory");
    { u64 t; asm volatile("s_memtime %0\n\ts_waitcnt lgkmcnt(0)" : "=s"(t)); t_start = (unsigned)t; }
    const int r0 = lane >> 3, pos = lane & 7;
    const unsigned v0 = r0 * 8192 + (((pos >> 1) ^ ((r0 >> 1) & 1)) * 32) + (pos & 1) * 16;
    const u16* src = p.wq + (size_t)(rg * 128) * 4096 + kvh * 512 + cg * 64;
    const unsigned v1 = (v0 ^ 64u) + 65536u, va = (dph & 8) ? v1 : v0, vb = (dph & 8) ? v0 : v1, la = (dph & 8) ? 1024u : 0u;
#pragma unroll 1
    for (int i = 0; i < 8; ++i) {
      const int ii = i ^ (dph & 7);
      dma16(src + (size_t)ii * 16 * 4096, va, LB + L_WQ + ii * 2048 + la);
      dma16(src + (size_t)ii * 16 * 4096, vb, LB + L_WQ + ii * 2048 + (la ^ 1024));
    }
  }
  const size_t kvbase = ((size_t)(b * 8 + kvh) * SK + (SK - WIN) + sp * 128) * 128;
  const unsigned char* kp = (const unsigned char*)(p.kc + kvbase);
  const unsigned char* vp = (const unsigned char*)(p.vc + kvbase);
  const int pk0 = (((wave - 1) * 8) ^ dph) & 31;
  unsigned ko = g * 256 + ((c ^ (4 * g)) * 16);
  PIN(ko);
  RBAR();
  {
    int wave1 = wave;
    asm volatile("" : "+s"(wave1));
    if (wave1 != 0) {
      const int w = wave - 1;
#define DMAK(i) dma16(kp + (pk0 ^ (i)) * 1024, ko ^ (((i) & 3) * 16), LB + L_K + w * 8192 + (i) * 1024)
#pragma unroll
      for (int i = 0; i < 8; ++i) DMAK(i);
    }
  }
  int wave2 = wave;
  asm volatile("" : "+s"(wave2));

  if (__builtin_expect(wave2 == 0, 1)) {
    if (lane < 8) LD(unsigned, LB + L_TAG + 4 + lane * 4) = 0u;
    if (lane == 8) LD(unsigned, LB + L_TAG + 48) = 0u;
    const int q4 = (lane >> 2) & 3, pp = lane & 3;
    unsigned tra = LB + L_WQ + (8 * g + q4) * 128 + pp * 8 + ((((q4 >> 1) & 1) | ((g & 1) << 1)) * 32) + ((dph >> 1) & 3) * 4096;
    unsigned qoff = WS_QG + (kvh * 4 + rg) * 8192 + (cg * 64 + lane) * 8;
    unsigned a_ln = LB + L_ACT + (lane >> 5) * ACT_PITCH + (lane & 31) * 8;
    unsigned a_af = LB + L_ACT + (c & 1) * ACT_PITCH + ((dph >> 1) & 3) * 64;
    unsigned af0 = a_af + g * 16, af1 = (((dph >> 1) & 3) ^ 1) * 64 + LB + L_ACT + (c & 1) * ACT_PITCH + g * 16;
    unsigned af2 = (((dph >> 1) & 3) ^ 2) * 64 + LB + L_ACT + (c & 1) * ACT_PITCH + g * 16, af3 = (((dph >> 1) & 3) ^ 3) * 64 + LB + L_ACT + (c & 1) * ACT_PITCH + g * 16;
    unsigned trs[16];
#pragma unroll
    for (int i = 0; i < 16; ++i) { trs[i] = tra ^ ((i & 3) * 32 + (i >> 2) * 4096); PIN(trs[i]); }
    PIN(qoff); PIN(a_ln); PIN(af0); PIN(af1); PIN(af2); PIN(af3);
    asm volatile("s_waitcnt vmcnt(16)" : "+v"(ep), "+v"(x0), "+v"(x1), "+v"(xs)::"memory");
    v4u af[4];
    {
      float mb, inv;
      ln_stats(x0, x1, mb, inv);
      const unsigned c0 = pk_bf16(bf_lo(xs.x) - mb, bf_hi(xs.x) - mb), c1 = pk_bf16(bf_lo(xs.y) - mb, bf_hi(xs.y) - mb);
      LD(v2u, a_ln) = v2u{pk_bf16(bf_lo(c0) * inv, bf_hi(c0) * inv), pk_bf16(bf_lo(c1) * inv, bf_hi(c1) * inv)};
      CBAR();
      af[0] = LD(v4u, af0); af[1] = LD(v4u, af1); af[2] = LD(v4u, af2); af[3] = LD(v4u, af3);
      CBAR();
    }
    tag = (__builtin_amdgcn_readfirstlane(ep) | 1u) + 1u;
    if (lane == 0) LD(unsigned, LB + L_TAG) = tag;
    const unsigned np0 = LD(unsigned, LB + L_TAG + 40);
    f32x4 acc[4] = {};
#pragma unroll
    for (int s = 0; s < 4; ++s) {
      if (s == 0) asm volatile("s_waitcnt vmcnt(12)" ::: "memory");
      if (s == 1) asm volatile("s_waitcnt vmcnt(8)" ::: "memory");
      if (s == 2) asm volatile("s_waitcnt vmcnt(4)" ::: "memory");
      if (s == 3) asm volatile("s_waitcnt vmcnt(0)" ::: "memory");
      v4u bf[4];
#pragma unroll
      for (int t = 0; t < 4; ++t) bf[t] = tr_frag(trs[4 * s + t], trs[4 * s + t] + 512);
      __builtin_amdgcn_sched_barrier(0);
#pragma unroll
      for (int t = 0; t < 4; ++t) acc[t] = mfma(af[s], bf[t], acc[t]);
      __builtin_amdgcn_sched_barrier(0);
    }
    {
      const float s0 = (g & 1) ? acc[1][0] : acc[0][0], s1 = (g & 1) ? acc[1][1] : acc[0][1];
      const float s2 = (g & 1) ? acc[3][0] : acc[2][0], s3 = (g & 1) ? acc[3][1] : acc[2][1];
      const v2u q0 = {U_((g & 2) ? s2 : s0), tag | np0}, q1 = {U_((g & 2) ? s3 : s1), tag | np0};
      __builtin_amdgcn_raw_buffer_store_b64(q0, rs, qoff, 0, SC1);
      __builtin_amdgcn_raw_buffer_store_b64(q1, rs, qoff, 4096, SC1);
    }
    RBAR();
    while ((unsigned)__builtin_amdgcn_s_memtime() - t_start < TLOOK) __builtin_amdgcn_s_sleep(1);
    BAR();
    RBAR();
    const u16* wsrc; unsigned wv0, wstep;
    if (j < 16) {
      wsrc = p.wo + (size_t)((kvh * 4 + (j >> 2)) * 128) * 512 + (j & 3) * 128;
      wv0 = g * 1024 + (((c >> 1) ^ g) * 32) + (c & 1) * 16; wstep = 4096;
    } else {
      wsrc = p.w1 + (kvh * 7 + (j - 16)) * 32;
      wv0 = (lane >> 2) * (FF * 2) + (((pp >> 1) ^ (lane >> 5)) * 32) + (pp & 1) * 16; wstep = 16 * FF * 2;
    }
    const unsigned wx = j < 16 ? 128u : 0u;
    const unsigned wv1 = wv0 + wstep, wv2 = (wv0 ^ wx) + 2 * wstep, wv3 = (wv0 ^ wx) + 3 * wstep;
#define W4(i_) do { const int i4 = (i_) ^ ((dph & 7) * 4); const unsigned char* s_ = (const unsigned char*)wsrc + (size_t)i4 * wstep; const unsigned l_ = LB + L_W + i4 * 1024; \
    dma16(s_, wv0, l_); dma16(s_, wv1, l_ + 1024); dma16(s_, wv2, l_ + 2048); dma16(s_, wv3, l_ + 3072); } while (0)
    const int wn1 = j < 16 ? 32 : WN1F;
    RBAR();
    npo = __builtin_amdgcn_readfirstlane(LD(unsigned, LB + L_TAG + 44));
    __builtin_amdgcn_s_setprio(0);
    if (j < 23) {
#pragma unroll 1
      for (int i = 0; i < wn1; i += 4) W4(i);
    }
    {
      const unsigned o_ml = c ? 0x7ffffff0u : pa_ml(kvh, g, b) + sp * 128;
      float mw, lw;
#pragma unroll 1
      for (;;) {
        const unsigned cnt = *(volatile LAS unsigned*)(LB + L_TAG + 4);
        mw = *(volatile LAS float*)(LB + L_MX + g * 16 + pp * 4);
        if (__builtin_amdgcn_readfirstlane(cnt) >= 4u) break;
        __builtin_amdgcn_s_sleep(1);
      }
      float M = vmax(mw, dpp<XOR1>(mw)); M = vmax(M, dpp<XOR2>(M));
      __builtin_amdgcn_raw_buffer_store_b64(v2u{U_(M), tag}, rs, o_ml, npo, SC1);
      const float fw = __builtin_amdgcn_exp2f(mw - M);
#pragma unroll 1
      for (;;) {
        const unsigned cnt = *(volatile LAS unsigned*)(LB + L_TAG + 16);
        lw = *(volatile LAS float*)(LB + L_LS + g * 16 + pp * 4);
        if (__builtin_amdgcn_readfirstlane(cnt) >= 4u) break;
        __builtin_amdgcn_s_sleep(1);
      }
      float S = lw * fw;
      S += dpp<XOR1>(S); S += dpp<XOR2>(S);
      __builtin_amdgcn_raw_buffer_store_b64(v2u{U_(S), tag}, rs, o_ml, npo + 2048, SC1);
    }
    if (j >= 16 && j < 23) {
      while (__builtin_amdgcn_readfirstlane(*(volatile LAS unsigned*)(LB + L_TAG + 12)) < 4u) __builtin_amdgcn_s_sleep(1);
#pragma unroll 1
      for (int i = WN1F; i < 32; i += 4) W4(i);
      {
        const u16* src2 = p.w2 + (size_t)((kvh * 7 + (j - 16)) * 32) * 512;
        const unsigned u0 = lane * 16, u1 = (u0 ^ 32u) + 1024u, u2 = (u0 ^ 64u) + 2048u, u3 = (u0 ^ 96u) + 3072u;
#pragma unroll 1
        for (int i = 0; i < 8; ++i) {
          const int i4 = i ^ (dph & 7);
          const unsigned char* s_ = (const unsigned char*)src2 + i4 * 4096; const unsigned l_ = LB + L_W + 32768 + i4 * 4096, x_ = (i4 & 2) * 64;
          dma16(s_, u0 ^ x_, l_); dma16(s_, u1 ^ x_, l_ + 1024); dma16(s_, u2 ^ x_, l_ + 2048); dma16(s_, u3 ^ x_, l_ + 3072);
        }
      }
    }
  } else {
    const int w = wave - 1;
    v4u kf[8], vf[8];
#define LDV(i) LDNT(vp + (pk0 ^ (i)) * 1024 + vo)
    unsigned vo = lane * 16;
    unsigned mk0 = (c & 2) ? 0u : ((c & 1) ? 0xffff0000u : 0x0000ffffu), mk1 = (c & 2) ? ((c & 1) ? 0xffff0000u : 0x0000ffffu) : 0u;
    unsigned a_q = LB + L_QS + (c >> 2) * QS_PITCH + g * 16;
    unsigned a_mw = LB + L_MX + (c >> 2) * 16 + w * 4, a_mr = LB + L_MX + g * 16, a_mo = LB + L_MX + g * 16 + w * 4;
    unsigned a_ow = LB + L_RED + w * RED_PITCH + g * 512 + c * 16;
    unsigned a_or = LB + L_RED + w * 512 + ((lane >> 1) & 1) * 256 + (lane >> 2) * 16 + (lane & 1) * 8;
    unsigned o_pa = pa_data(kvh, w, b) + sp * 512 + lane * 8;
    unsigned a_cnt = LB + L_TAG + 4, one = 1u;
    PIN(vo); PIN(one); PIN(mk0); PIN(mk1); PIN(a_q); PIN(a_mw); PIN(a_mr); PIN(a_mo); PIN(a_ow); PIN(a_or); PIN(o_pa); PIN(a_cnt);
    unsigned a_k = LB + L_K + w * 8192 + (c & 3) * 1024 + (c >> 2) * 256 + ((g ^ c) * 16);
    PIN(a_k);
#pragma unroll
    for (int i = 0; i < NV0; ++i) vf[i] = LDV(i);
    RBAR();
#pragma unroll
    for (int i = NV0; i < NV1; ++i) vf[i] = LDV(i);
    unsigned goff = WS_QG + kvh * 4 * 8192 + b * 4096 + w * 1024 + lane * 16;
    unsigned a_qw = LB + L_QS + w * QS_PITCH + lane * 4;
    PIN(goff); PIN(a_qw);
    asm volatile("s_waitcnt vmcnt(%0)" ::"i"(NV1) : "memory");
#pragma unroll
    for (int i = 0; i < 8; ++i) kf[i] = LD(v4u, (a_k ^ ((i & 3) * 64)) + (i >> 2) * 4096);
#pragma unroll
    for (int i = 0; i < 8; ++i) PIN(kf[i]);
    RBAR();
    v4u d[4];
#define QLOAD() _Pragma("unroll") for (int i = 0; i < 4; ++i) d[i] = __builtin_amdgcn_raw_buffer_load_b128(rs, goff, i * 8192, SC1)
    QLOAD();
    RBAR();
#pragma unroll
    for (int i = NV1; i < 8; ++i) vf[i] = LDV(i);
    tag = LD(unsigned, a_cnt - 4);
    PIN(tag);
    __builtin_amdgcn_s_setprio(2);
#define QBAD() ({ unsigned bad = 0; _Pragma("unroll") for (int i = 0; i < 4; ++i) bad = tagbad(bad, d[i], tag); ANY(bad >> 1); })
#define QPUT() do { LD(unsigned, a_qw) = pk_bf16((F_(d[0].x) + F_(d[1].x)) + (F_(d[2].x) + F_(d[3].x)), (F_(d[0].z) + F_(d[1].z)) + (F_(d[2].z) + F_(d[3].z))); \
    if (w == 0) asm volatile("s_mov_b64 exec, 1\n\tds_write_b32 %0, %1 offset:40\n\ts_mov_b64 exec, -1" ::"v"(a_cnt), "v"((d[0].y & d[1].y & d[2].y & d[3].y & 1u) << 13) : "memory"); } while (0)
    QPUT();
    if (__builtin_expect(QBAD(), 0)) {
#pragma unroll 1
      for (unsigned spin = 0; spin < SPIN_CAP && QBAD(); ++spin) {
        CBAR();
        QLOAD();
      }
      __builtin_amdgcn_s_waitcnt(0x0F70);
      QPUT();
    }
    BAR();
    v4u qf[4];
#pragma unroll
    for (int s = 0; s < 4; ++s) qf[s] = LD(v4u, a_q + s * 64);
    npo = LD(unsigned, a_cnt + 40);
    __builtin_amdgcn_sched_barrier(0);
    f32x4 sc[2] = {};
#pragma unroll
    for (int s = 0; s < 4; ++s) { sc[0] = mfma(kf[s], qf[s], sc[0]); sc[1] = mfma(kf[4 + s], qf[s], sc[1]); __builtin_amdgcn_sched_barrier(0); }
    o_pa += npo;
    float sv[8];
#pragma unroll
    for (int i = 0; i < 4; ++i) {
      const unsigned r = pk_bf16(sc[i >> 1][(i & 1) * 2], sc[i >> 1][(i & 1) * 2 + 1]);
      const f32x2 t2 = f32x2{bf_lo(r), bf_hi(r)} * 0.08838834764831845f;
      const unsigned t = pk_bf16(t2[0], t2[1]);
      sv[2 * i] = bf_lo(t); sv[2 * i + 1] = bf_hi(t);
    }
    const float mw = xrows_max(vmax3(vmax3(sv[0], sv[1], sv[2]), vmax3(sv[3], sv[4], sv[5]), vmax(sv[6], sv[7]))) * LOG2E;
    asm volatile("s_mov_b64 exec, 0x1111\n\tds_write_b32 %0, %1\n\ts_mov_b64 exec, 1\n\tds_add_u32 %2, %3\n\ts_mov_b64 exec, -1"
                 ::"v"(a_mw), "v"(mw), "v"(a_cnt), "v"(one) : "memory");
    __builtin_amdgcn_sched_barrier(0);
    float e[8];
    f32x4 olo = {}, ohi = {};
    unsigned cnt2; f32x4 m0; float mo;
#pragma unroll
    for (int t = 0; t < 8; ++t) {
      e[t] = __builtin_amdgcn_exp2f(fmaf(sv[t], LOG2E, -mw));
      const unsigned pk = pk_bf16(e[t], e[t]);
      const v2u pa = v2u{pk & mk0, pk & mk1};
      olo = mfma16(pa, v2u{vf[t].x, vf[t].y}, olo); ohi = mfma16(pa, v2u{vf[t].z, vf[t].w}, ohi);
      if (t == 3) {
        __builtin_amdgcn_sched_barrier(0);
        cnt2 = *(volatile LAS unsigned*)(a_cnt); m0 = *(volatile LAS f32x4*)(a_mr); mo = *(volatile LAS float*)(a_mo);
        __builtin_amdgcn_sched_barrier(0);
      }
    }
    float fac = __builtin_amdgcn_exp2f(mo - vmax3(vmax(m0[0], m0[1]), m0[2], m0[3]));
    PIN(fac); PIN(olo); PIN(ohi);
    if (__builtin_expect(__builtin_amdgcn_readfirstlane(cnt2) < 4u, 0)) {
      do {
        cnt2 = *(volatile LAS unsigned*)(a_cnt);
        m0 = *(volatile LAS f32x4*)(a_mr);
      } while (__builtin_amdgcn_readfirstlane(cnt2) < 4u);
      fac = __builtin_amdgcn_exp2f(mo - vmax3(vmax(m0[0], m0[1]), m0[2], m0[3]));
    }
    LD(f32x4, a_ow) = olo * fac;
    LD(f32x4, a_ow + 256) = ohi * fac;
    asm volatile("s_mov_b64 exec, 1\n\tds_add_u32 %0, %1 offset:4\n\ts_mov_b64 exec, -1" ::"v"(a_cnt), "v"(one) : "memory");
    {
      const float lw = x32_sum(x16_sum(((e[0] + e[1]) + (e[2] + e[3])) + ((e[4] + e[5]) + (e[6] + e[7]))));
      asm volatile("s_mov_b64 exec, 0x1111\n\tds_write_b32 %0, %1 offset:128\n\ts_mov_b64 exec, 1\n\tds_add_u32 %2, %3 offset:12\n\ts_mov_b64 exec, -1"
                   ::"v"(a_mw), "v"(lw), "v"(a_cnt), "v"(one) : "memory");
    }
    {
      f32x2 r0, r1, r2, r3;
#pragma unroll 1
      for (;;) {
        const unsigned cnt = *(volatile LAS unsigned*)(a_cnt + 4);
        r0 = *(volatile LAS f32x2*)(a_or); r1 = *(volatile LAS f32x2*)(a_or + RED_PITCH);
        r2 = *(volatile LAS f32x2*)(a_or + 2 * RED_PITCH); r3 = *(volatile LAS f32x2*)(a_or + 3 * RED_PITCH);
        if (__builtin_amdgcn_readfirstlane(cnt) >= 4u) break;
      }
      const f32x2 a = (r0 + r1) + (r2 + r3);
      __builtin_amdgcn_raw_buffer_store_b64(v2u{pk_bf16(a[0], a[1]), tag}, rs, o_pa, 0, SC1);
    }
    asm volatile("s_mov_b64 exec, 1\n\tds_add_u32 %0, %1 offset:8\n\ts_mov_b64 exec, -1" ::"v"(a_cnt), "v"(one) : "memory");
  }

  if (j < 16) {
    const int h = kvh * 4 + (j >> 2), cq = j & 3;
    if (wave == 0) {
      const unsigned mlo = pa_ml(kvh, j >> 2, g & 1) + c * 128 + npo;
      v2u dm, dl;
      asm volatile("buffer_load_dwordx2 %0, %2, %3, 0 offen sc1\n\tbuffer_load_dwordx2 %1, %2, %3, 0 offen offset:2048 sc1" : "=&v"(dm), "=&v"(dl) : "v"(mlo), "s"(rs) : "memory");
      asm volatile("s_waitcnt vmcnt(2)" ::: "memory");
      if (lane == 0) *(volatile LAS unsigned*)(LB + L_TAG + 20) = 1u;
      asm volatile("s_waitcnt vmcnt(0)" : "+v"(dm), "+v"(dl)::"memory");
#pragma unroll 1
      for (unsigned spin = 0; spin < SPIN_CAP && ANY(dm.y ^ tag); ++spin) {
        CBAR();
        dm = __builtin_amdgcn_raw_buffer_load_b64(rs, mlo, 0, SC1); dl = __builtin_amdgcn_raw_buffer_load_b64(rs, mlo + 2048, 0, SC1);
      }
      v2u dl2 = __builtin_amdgcn_raw_buffer_load_b64(rs, mlo + 2048, 0, SC1);
      const float m = F_(dm.x);
      float M = vmax(m, dpp<XOR1>(m)); M = vmax(M, dpp<XOR2>(M)); M = vmax(M, dpp<HMIR>(M)); M = vmax(M, dpp<ROR8>(M));
      const float ex = __builtin_amdgcn_exp2f(m - M);
      const unsigned wb = pk_bf16(ex, ex);
      if (g < 2) *(volatile LAS unsigned*)(LB + L_WN + g * 64 + (c & 3) * 16 + (c >> 2) * 4) = wb;
      if (lane == 0) *(volatile LAS unsigned*)(LB + L_TAG + 24) = 1u;
      if (ANY(dl.y ^ tag)) {
        dl = dl2;
#pragma unroll 1
        for (unsigned spin = 0; spin < SPIN_CAP && ANY(dl.y ^ tag); ++spin) { CBAR(); dl = __builtin_amdgcn_raw_buffer_load_b64(rs, mlo + 2048, 0, SC1); }
      }
      float S = bf_lo(wb) * F_(dl.x);
      S += dpp<XOR1>(S); S += dpp<XOR2>(S); S += dpp<HMIR>(S); S += dpp<ROR8>(S);
      if (g < 2 && c == 0) *(volatile LAS float*)(LB + L_TAG + 32 + g * 4) = __builtin_amdgcn_rcpf(S);
      if (lane == 0) *(volatile LAS unsigned*)(LB + L_TAG + 28) = 1u;
      return;
    }
    const int w = wave - 1;
    const int ob = w >> 1, dh = w & 1;
    unsigned off = pa_data(kvh, j >> 2, ob) + g * 512 + dh * 256 + c * 16 + npo;
    v4u d[4];
    unsigned a_ow = LB + L_ACT + ob * ACT_PITCH + dh * 128 + c * 8;
    unsigned a_or = LB + L_ACT + (c & 1) * ACT_PITCH + g * 16;
    unsigned o_pb = g > 1 ? 0x7ffffff0u : pb_inbox(cq * 8 + 2 * w + g) + h * 128 + c * 8;
    unsigned a_wn = LB + L_WN + ob * 64 + g * 16, a_fl = LB + L_TAG + 20, a_inv = LB + L_TAG + 32;
    const int q4 = (lane >> 2) & 3, pp = lane & 3;
    unsigned a_wf = LB + L_W + (8 * g + q4) * 256 + (((2 * w) ^ (q4 | ((g & 1) << 2))) * 32) + pp * 8;
    unsigned mk0 = (c & 2) ? 0u : ((c & 1) ? 0xffff0000u : 0x0000ffffu), mk1 = (c & 2) ? ((c & 1) ? 0xffff0000u : 0x0000ffffu) : 0u;
    unsigned one = 1u;
    PIN(off); PIN(a_ow); PIN(a_or); PIN(o_pb); PIN(a_wn); PIN(a_fl); PIN(a_wf); PIN(a_inv); PIN(mk0); PIN(mk1); PIN(one);
#define OLOAD() _Pragma("unroll") for (int i = 0; i < 4; ++i) d[i] = __builtin_amdgcn_raw_buffer_load_b128(rs, off + (i & 1) * 2048, (i >> 1) * 4096, SC1)
#define OOK() ({ unsigned bad = 0; _Pragma("unroll") for (int i = 0; i < 4; ++i) bad = tagbad(bad, d[i], tag); !ANY(bad); })
    OLOAD();
    while (__builtin_amdgcn_readfirstlane(*(volatile LAS unsigned*)(a_fl)) == 0u) {}
    CBAR();
    v4u wf[8];
#pragma unroll
    for (int s = 0; s < 8; ++s) wf[s] = tr_frag((a_wf ^ ((s & 1) * 32)) + (s >> 1) * 8192, (a_wf ^ ((s & 1) * 32)) + (s >> 1) * 8192 + 1024);
    v4u wn; unsigned fl;
#pragma unroll 1
    for (unsigned spin = 0; spin < SPIN_CAP; ++spin) {
      fl = *(volatile LAS unsigned*)(a_fl + 4);
      wn = *(volatile LAS v4u*)(a_wn);
      if (OOK()) break;
      CBAR(); OLOAD();
    }
    while (__builtin_amdgcn_readfirstlane(fl) == 0u) {
      fl = *(volatile LAS unsigned*)(a_fl + 4);
      wn = *(volatile LAS v4u*)(a_wn);
    }
    v2u wa[4];
#pragma unroll
    for (int i = 0; i < 4; ++i) wa[i] = v2u{wn[i] & mk0, wn[i] & mk1};
    {
      f32x4 o = {};
#pragma unroll
      for (int i = 0; i < 4; ++i) o = mfma16(wa[i], v2u{d[i].x, d[i].z}, o);
      if (g == 0) LD(v2u, a_ow) = v2u{pk_bf16(o[0], o[1]), pk_bf16(o[2], o[3])};
    }
    asm volatile("s_mov_b64 exec, 1\n\tds_add_u32 %0, %1 offset:28\n\ts_mov_b64 exec, -1" ::"v"(a_fl), "v"(one) : "memory");
    f32x4 acc0 = {}, acc1 = {};
    v4u of[4];
#pragma unroll 1
    for (;;) {
      const unsigned cnt = *(volatile LAS unsigned*)(a_fl + 28);
#pragma unroll
      for (int s = 0; s < 4; ++s) of[s] = *(volatile LAS v4u*)(a_or + s * 64);
      if (__builtin_amdgcn_readfirstlane(cnt) >= 4u) break;
    }
    unsigned fi = *(volatile LAS unsigned*)(a_fl + 8);
    f32x2 inv = *(volatile LAS f32x2*)(a_inv);
    __builtin_amdgcn_sched_barrier(0);
#pragma unroll
    for (int s = 0; s < 4; ++s) { acc0 = mfma(of[s], wf[2 * s], acc0); acc1 = mfma(of[s], wf[2 * s + 1], acc1); }
    float y0 = g ? acc1[0] : acc0[0], y1 = g ? acc1[1] : acc0[1];
    PIN(y0); PIN(y1);
    while (__builtin_expect(__builtin_amdgcn_readfirstlane(fi) == 0u, 0)) { fi = *(volatile LAS unsigned*)(a_fl + 8); inv = *(volatile LAS f32x2*)(a_inv); }
    __builtin_amdgcn_raw_buffer_store_b64(v2u{pk_bf16(y0 * inv[0], y1 * inv[1]), tag}, rs, o_pb, 0, SC1);
  } else if (j < 23) {
    const int f = kvh * 7 + (j - 16);
    if (wave == 0) {
      asm volatile("s_waitcnt vmcnt(0)" ::: "memory");
      RBAR();
      RBAR();
      unsigned a_h = LB + L_H + ((lane >> 2) & 1) * H_PITCH + (lane >> 3) * 32 + (lane & 3) * 8;
      unsigned a_st = LB + L_WN + ((lane >> 2) & 1) * 8;
      PIN(a_h); PIN(a_st);
      v4u d[4];
      poll<4, 1024>(rs, WS_HG + (kvh & 1) * 16384 + npo + ((kvh >> 1) & 1) * 4096 + lane * 16, tag, d);
#pragma unroll
      for (int i = 0; i < 4; ++i) LD(v2u, a_h + i * 256) = v2u{d[i].x, d[i].z};
      BAR();
      PIN(d[0]); PIN(d[1]); PIN(d[2]); PIN(d[3]);
      float s0 = 0.f, s1 = 0.f, q0 = 0.f, q1 = 0.f;
#pragma unroll
      for (int i = 0; i < 4; ++i) {
        s0 = dot2(d[i].x, 0x3f803f80u, s0); s1 = dot2(d[i].z, 0x3f803f80u, s1);
        q0 = dot2(d[i].x, d[i].x, q0); q1 = dot2(d[i].z, d[i].z, q1);
      }
      float t = x16_sum2(s0 + s1, q0 + q1);
      t += dpp<XOR1>(t); t += dpp<XOR2>(t); t += dpp<ROR8>(t);
      t = x32_sum(t) * (1.0f / 512.0f);
      auto u = __builtin_amdgcn_permlane16_swap(U_(t), U_(t), false, false);
      const float mean = F_(u[0]);
      const float var = fmaxf(fmaf(-mean, mean, F_(u[1])), 0.f);
      const float inv = __builtin_amdgcn_rcpf(rbf(__builtin_amdgcn_sqrtf(rbf(rbf(var) + 1e-5f))));
      LD(f32x2, a_st) = f32x2{rbf(mean), inv};
      BAR();
      return;
    }
    const int w = wave - 1;
    float bias1 = bf_lo(p.b1[f * 32 + (lane & 31)]);
    RBAR();
    v4u w1f[8], w2f[8];
    {
      const int q4 = (lane >> 2) & 3, pp = lane & 3;
      const unsigned b1a = LB + L_W + (128 * w + 8 * g + q4) * 64 + ((g & 1) * 32) + pp * 8;
#pragma unroll
      for (int s = 0; s < 8; ++s) w1f[s] = tr_frag((b1a ^ ((s & 1) * 32)) + (s >> 1) * 2048, (b1a ^ ((s & 1) * 32)) + (s >> 1) * 2048 + 256);
      const unsigned b2a = LB + L_W + 32768 + (8 * g + q4) * 1024 + w * 256 + ((q4 | ((g & 1) << 2)) * 32) + pp * 8;
#pragma unroll
      for (int t = 0; t < 8; ++t) w2f[t] = tr_frag(b2a ^ (t * 32), (b2a ^ (t * 32)) + 4096);
    }
    const f32x4 z4 = {};
    unsigned a_zw = LB + L_ZP + ((g & 1) * 32 + (g >> 1) * 16 + c) * 16 + w * 4;
    unsigned a_zr = LB + L_ZP + lane * 16;
    unsigned a_ha = LB + L_H + (c & 1) * H_PITCH + w * 256 + g * 16;
    unsigned a_st = LB + L_WN + (lane >> 5) * 8;
    unsigned a_gw = LB + L_GA + w * 160 + (lane >> 5) * 80 + (lane & 31) * 2, a_gr = LB + L_GA + w * 160 + (c & 1) * 80 + g * 16;
    unsigned o_pd = WS_PD + (8 * w + g) * 8192 + f * 128 + c * 8;
    float csum;
    {
      const v4u ones = {0x3f803f80u, 0x3f803f80u, 0x3f803f80u, 0x3f803f80u};
      f32x4 c0 = z4, c1 = z4;
#pragma unroll
      for (int s = 0; s < 4; ++s) { c0 = mfma(ones, w1f[2 * s], c0); c1 = mfma(ones, w1f[2 * s + 1], c1); }
      LD(float, a_zw) = (g & 2) ? c1[0] : c0[0];
      BAR();
      const f32x4 a = LD(f32x4, a_zr);
      csum = (a[0] + a[1]) + (a[2] + a[3]);
      asm volatile("" : "+v"(csum), "+v"(w2f[0]), "+v"(w2f[1]), "+v"(w2f[2]), "+v"(w2f[3]), "+v"(w2f[4]), "+v"(w2f[5]), "+v"(w2f[6]), "+v"(w2f[7]), "+v"(bias1));
      PIN(a_zw); PIN(a_zr); PIN(a_ha); PIN(a_st); PIN(a_gw); PIN(a_gr); PIN(o_pd);
    }
    BAR();
    {
      v4u ha[4];
#pragma unroll
      for (int s = 0; s < 4; ++s) ha[s] = LD(v4u, a_ha + s * 64);
      __builtin_amdgcn_sched_barrier(0);
      f32x4 z0 = z4, z1 = z4;
#pragma unroll
      for (int s = 0; s < 4; ++s) { z0 = mfma(ha[s], w1f[2 * s], z0); z1 = mfma(ha[s], w1f[2 * s + 1], z1); }
      const float t0 = (g & 1) ? z0[1] : z0[0], t1 = (g & 1) ? z1[1] : z1[0];
      LD(float, a_zw) = (g & 2) ? t1 : t0;
    }
    BAR();
    {
      const f32x4 a = LD(f32x4, a_zr);
      const f32x2 st = LD(f32x2, a_st);
      const float zr = (a[0] + a[1]) + (a[2] + a[3]);
      const float z = rbf(rbf(fmaf(-st[0], csum, zr) * st[1]) + bias1);
      const float t = fmaf(z * z, 0.044715f * 0.7978845608028654f * 2.f * LOG2E, 0.7978845608028654f * 2.f * LOG2E);
      const float e = __builtin_amdgcn_exp2f(z * t);
      const float gl = fmaf(-z, __builtin_amdgcn_rcpf(e + 1.0f), z);
      LD(u16, a_gw) = (u16)pk_bf16(gl, gl);
    }
    CBAR();
    const v4u ga = LD(v4u, a_gr);
#pragma unroll
    for (int u = 0; u < 2; ++u) {
      const f32x4 y0 = mfma(ga, w2f[4 * u], z4), y1 = mfma(ga, w2f[4 * u + 1], z4), y2 = mfma(ga, w2f[4 * u + 2], z4), y3 = mfma(ga, w2f[4 * u + 3], z4);
      const unsigned p0 = pk_bf16(y0[0], y0[1]), p1 = pk_bf16(y1[0], y1[1]), p2 = pk_bf16(y2[0], y2[1]), p3 = pk_bf16(y3[0], y3[1]);
      __builtin_amdgcn_raw_buffer_store_b64(v2u{(g & 2) ? ((g & 1) ? p3 : p2) : ((g & 1) ? p1 : p0), tag}, rs, o_pd, u * 4 * 8192, SC1);
    }
  } else if (j < 31 && wave == 0) {
    const unsigned np = npo >> 13;
    const unsigned role = claim_role(p.ws, (((j - 23) * 4 + (kvh & 3)) * 2) | np, tag);
    const int r = role & 31, n = lane & 7, pg = lane >> 3;
    if (j < 27) {
      unsigned xr = *(const unsigned*)(p.x + (g & 1) * 512 + r * 16 + 2 * n);
      unsigned o_hg = WS_HG + ((lane >> 3) & 1) * 8192 + (g >> 1) * 4096 + r * 128 + ((g & 1) * 8 + n) * 8;
      PIN(xr); PIN(o_hg);
      const f32x4 a = poll_sum<4>(rs, pb_inbox(r) + lane * 16, tag);
      float t = x32_sum2(x16_sum2(a[0], a[1]), x16_sum2(a[2], a[3]));
      t += dpp<ROR8>(t);
      auto sw = __builtin_amdgcn_permlane32_swap(U_(t), U_(t), false, false);
      const unsigned ar = pk_bf16(F_(sw[0]), F_(sw[1]));
      const unsigned pk = pk_bf16(bf_lo(xr) + bf_lo(ar), bf_hi(xr) + bf_hi(ar));
      __builtin_amdgcn_raw_buffer_store_b64(v2u{pk, tag}, rs, o_hg, 0, SC1);
      __builtin_amdgcn_raw_buffer_store_b64(v2u{pk, tag}, rs, o_hg, 16384, SC1);
      __builtin_amdgcn_raw_buffer_store_b64(v2u{pk, tag}, rs, o_hg, 32768, SC1);
      if (r == 0 && lane == 0) __builtin_amdgcn_raw_buffer_store_b32(tag, rs, WS_EPOCH, 0, SC1);
    } else {
      unsigned b2r = *(const unsigned*)(p.b2 + r * 16 + 2 * n);
      v2u hg;
      const unsigned hoff = WS_HG + 32768 + np * 8192 + r * 128 + ((g & 1) * 8 + n) * 8;
#pragma unroll 1
      for (unsigned spin = 0; spin < SPIN_CAP; ++spin) {
        CBAR();
        hg = __builtin_amdgcn_raw_buffer_load_b64(rs, hoff, 0, SC1);
        if (__all(hg.y == tag)) break;
        __builtin_amdgcn_s_sleep(2);
      }
      unsigned o_out = ((g & 1) * 512 + r * 16 + 2 * n) * 2;
      PIN(b2r); PIN(o_out);
      const f32x4 a = poll_sum<7>(rs, WS_PD + r * 8192 + lane * 16, tag);
      float t = x32_sum2(x16_sum2(a[0], a[1]), x16_sum2(a[2], a[3]));
      t += dpp<ROR8>(t);
      auto sw = __builtin_amdgcn_permlane32_swap(U_(t), U_(t), false, false);
      const unsigned ar = pk_bf16(F_(sw[0]), F_(sw[1]));
      const unsigned yr = pk_bf16(bf_lo(ar) + bf_lo(b2r), bf_hi(ar) + bf_hi(b2r));
      const unsigned pk = pk_bf16(bf_lo(hg.x) + bf_lo(yr), bf_hi(hg.x) + bf_hi(yr));
      if ((lane & 40) == 0) *(unsigned*)((unsigned char*)p.out + o_out) = pk;
    }
  }
}

}

extern "C" void kernel_launch(void* const* d_in, const int* in_sizes, int n_in,
                              void* d_out, int out_size, void* d_ws, size_t ws_size,
                              hipStream_t stream) {
  Params p;
  p.x = (const u16*)d_in[0]; p.kc = (const u16*)d_in[1]; p.vc = (const u16*)d_in[2];
  p.wq = (const u16*)d_in[3]; p.wo = (const u16*)d_in[4];
  p.w1 = (const u16*)d_in[6]; p.b1 = (const u16*)d_in[7]; p.w2 = (const u16*)d_in[8]; p.b2 = (const u16*)d_in[9];
  p.out = (u16*)d_out; p.ws = (unsigned char*)(((uintptr_t)d_ws + 16383) & ~(uintptr_t)16383);
  (void)hipFuncSetAttribute((const void*)fused_decode, hipFuncAttributeMaxDynamicSharedMemorySize, LDS_BYTES);
  hipLaunchKernelGGL(fused_decode, dim3(256), dim3(NTHR), LDS_BYTES, stream, p);
}
```
